```python
import torch

def _softmax(x, axis=-1):
    return torch.softmax(x, dim=axis)
def _gelu(x):
    return torch.nn.functional.gelu(x, approximate="tanh")
def _silu(x):
    return torch.nn.functional.silu(x)
def _sigmoid(x):
    return torch.sigmoid(x)
def _layernorm(x, axis=-1, eps=1e-5):
    m = x.mean(dim=axis, keepdim=True)
    v = x.var(dim=axis, keepdim=True, unbiased=False)
    return (x - m) / torch.sqrt(v + eps)
def _scatter_add(tgt, idx, src, axis=0):
    return tgt.clone().index_add_(axis, idx, src)
def _segment_sum(src, seg, n):
    out = torch.zeros((n,) + src.shape[1:], dtype=src.dtype)
    return out.index_add_(0, seg, src)
def _pad(x, pw):
    flat = tuple(v for pair in reversed(pw) for v in pair)
    return torch.nn.functional.pad(x, flat)

kernel_name = 'concat_heads__spectral_gfnet__ssm_conv_s5__prompt'

def setup_inputs():

    _w = (lambda a: (lambda t: t.to(torch.bfloat16) if t.is_floating_point() else t)(torch.as_tensor(a)))
    def _A_setup():
        B, S, D = 1, 2048, 1024
        Df = D // 2 + 1
        return {
            "x": _w((lambda *s: torch.randn(*s, dtype=torch.bfloat16) * 0.05)(B, S, D)),
            "w_r": _w((lambda *s: torch.randn(*s, dtype=torch.bfloat16) * 0.8)(S, Df) * 0.02),
            "w_i": _w((lambda *s: torch.randn(*s, dtype=torch.bfloat16) * 0.8)(S, Df) * 0.02),
        }
    _A_in = _A_setup()

    def _B_setup():
        B, S, D = 1, 2048, 1024
        P = D
        Lambda_re = -0.5 * (lambda _x: torch.exp(torch.as_tensor(_x)))((lambda *s: torch.randn(*s, dtype=torch.bfloat16) * 0.2)(P))
        Lambda_im = (lambda *s: torch.randn(*s, dtype=torch.bfloat16) * 0.4)(P) * 1.0
        B_re = (lambda *s: torch.randn(*s, dtype=torch.bfloat16) * 0.2)(P, D) * (D ** -0.5)
        B_im = (lambda *s: torch.randn(*s, dtype=torch.bfloat16) * 0.2)(P, D) * (D ** -0.5)
        C_re = (lambda *s: torch.randn(*s, dtype=torch.bfloat16) * 0.2)(D, P) * (P ** -0.5)
        C_im = (lambda *s: torch.randn(*s, dtype=torch.bfloat16) * 0.2)(D, P) * (P ** -0.5)
        log_dt = (lambda *s: torch.randn(*s, dtype=torch.bfloat16) * 0.2)(P) * 0.3 - 2.0
        Dskip = (lambda *s: torch.randn(*s, dtype=torch.bfloat16) * 0.05)(D)
        return {
            "x": _w((lambda *s: torch.randn(*s, dtype=torch.bfloat16) * 0.05)(B, S, D)),
            "Lambda_re": _w(Lambda_re),
            "Lambda_im": _w(Lambda_im),
            "B_re": _w(B_re),
            "B_im": _w(B_im),
            "C_re": _w(C_re),
            "C_im": _w(C_im),
            "log_dt": _w(log_dt),
            "Dskip": _w(Dskip),
        }
    _B_in = _B_setup()

    return {
        "A_x": _A_in["x"],
        "A_w_r": _A_in["w_r"],
        "A_w_i": _A_in["w_i"],
        "B_x": _B_in["x"],
        "B_Lambda_re": _B_in["Lambda_re"],
        "B_Lambda_im": _B_in["Lambda_im"],
        "B_B_re": _B_in["B_re"],
        "B_B_im": _B_in["B_im"],
        "B_C_re": _B_in["C_re"],
        "B_C_im": _B_in["C_im"],
        "B_log_dt": _B_in["log_dt"],
        "B_Dskip": _B_in["Dskip"],
    }

def reference(A_x, A_w_r, A_w_i, B_x, B_Lambda_re, B_Lambda_im, B_B_re, B_B_im, B_C_re, B_C_im, B_log_dt, B_Dskip):
    def _A_ref(x, w_r, w_i):
        B, S, D = x.shape
        X = (lambda x, axes=(-2,-1): torch.fft.rfft2(x.float(), dim=axes))(x, axes=(-2, -1))
        W = w_r + 1j * w_i
        X = X * W
        y = (lambda x, s, axes=(-2,-1): torch.fft.irfft2(x, s=s, dim=axes).to(torch.bfloat16))(X, (S, D), axes=(-2, -1))
        return y

    def _B_ref(x, Lambda_re, Lambda_im, B_re, B_im, C_re, C_im, log_dt, Dskip):
        Bsz, L, H = x.shape
        Lam = Lambda_re + 1j * Lambda_im
        Bmat = B_re + 1j * B_im
        Cmat = C_re + 1j * C_im
        dt = torch.exp(log_dt)
        Lam_bar = torch.exp(dt * Lam)
        B_bar = ((Lam_bar - 1.0) / Lam)[:, None] * Bmat
        Bu = torch.einsum("bld,pd->blp", x + 0j, B_bar)
        h = torch.zeros_like(Bu[:, 0, :])
        hs = []
        for t in range(L):
            h = Lam_bar[None, :] * h + Bu[:, t, :]
            hs.append(h)
        hseq = torch.stack(hs, 1)
        y = 2.0 * torch.einsum("blp,dp->bld", hseq, Cmat).real + x * Dskip[None, None, :]
        return y

    a = _A_ref(A_x, A_w_r, A_w_i)
    b = _B_ref(B_x, B_Lambda_re, B_Lambda_im, B_B_re, B_B_im, B_C_re, B_C_im, B_log_dt, B_Dskip)
    return torch.concatenate([a, b], axis=-1)
```

```cpp
#include <hip/hip_runtime.h>
#include <stdint.h>

#define XSTR2(x) #x
#define XSTR(x) XSTR2(x)
namespace {

#if defined(__HIP_DEVICE_COMPILE__) && __HIP_DEVICE_COMPILE__
#pragma clang attribute push(__attribute__((target("no-packed-fp32-ops"))), apply_to = function)
#endif
__device__ __forceinline__ int tid_x() { return __builtin_amdgcn_workitem_id_x(); }
__device__ __forceinline__ int bid_x() { return __builtin_amdgcn_workgroup_id_x(); }

typedef float cf __attribute__((ext_vector_type(2)));
typedef short bf16x8 __attribute__((ext_vector_type(8)));
typedef short s16x4 __attribute__((ext_vector_type(4)));
typedef __bf16 b16x2 __attribute__((ext_vector_type(2)));
typedef float f32x4 __attribute__((ext_vector_type(4)));
typedef uint32_t u32x4 __attribute__((ext_vector_type(4)));
typedef uint32_t u32x2 __attribute__((ext_vector_type(2)));
typedef unsigned short u16;
typedef unsigned long long u64;
typedef __attribute__((address_space(3))) uint32_t lds_u32;

__device__ __forceinline__ float u2f(uint32_t u) { return __builtin_bit_cast(float, u); }
__device__ __forceinline__ uint32_t f2u(float f) { return __builtin_bit_cast(uint32_t, f); }
__device__ __forceinline__ float bf2f(u16 b) { return u2f((uint32_t)b << 16); }
__device__ __forceinline__ float rbf(float f) { return bf2f(__builtin_bit_cast(u16, (__bf16)f)); }
__device__ __forceinline__ uint32_t pk_bf(float a, float b) {
  const b16x2 h = b16x2{(__bf16)a, (__bf16)b};
  return __builtin_bit_cast(uint32_t, h);
}
__device__ __forceinline__ cf fin(cf v) {
  float x = v.x, y = v.y;
  asm("" : "+v"(x), "+v"(y));
  return cf{x, y};
}
__device__ __forceinline__ cf cmul(cf a, cf b) {
  return cf{__builtin_fmaf(a.x, b.x, -(a.y * b.y)), __builtin_fmaf(a.x, b.y, a.y * b.x)};
}
__device__ __forceinline__ cf cmad(cf a, cf b, cf c) {
  return cf{__builtin_fmaf(a.x, b.x, __builtin_fmaf(-a.y, b.y, c.x)), __builtin_fmaf(a.x, b.y, __builtin_fmaf(a.y, b.x, c.y))};
}
__device__ __forceinline__ cf cisr(int k, float inv_n) {
  const float r = (float)k * inv_n;
  return cf{__builtin_amdgcn_cosf(r), -__builtin_amdgcn_sinf(r)};
}
template <int N> __device__ __forceinline__ cf cis(int k) { return cisr(k, 1.0f / N); }
template <int N> __device__ __forceinline__ void wait_vm() { asm volatile("s_waitcnt vmcnt(%0)" ::"n"(N) : "memory"); }
__device__ __forceinline__ void wait_lgkm0() { asm volatile("s_waitcnt lgkmcnt(0)" ::: "memory"); }
__device__ __forceinline__ void glds2_dev(const void* sb, uint32_t v0, uint32_t lds_dst) {
  uint32_t keep;
  asm volatile(
      "s_mov_b32 %0, m0\n\ts_mov_b32 m0, %3\n\ts_nop 0\n\tglobal_load_lds_dwordx4 %1, %2 sc1\n\t"
      "global_load_lds_dwordx4 %1, %2 offset:1024 sc1\n\ts_mov_b32 m0, %0"
      : "=&s"(keep)
      : "v"(v0), "s"(sb), "s"(lds_dst)
      : "memory");
}
__device__ __forceinline__ void gdev2(const void* sb, uint32_t v0, uint32_t v1, uint32_t lds_dst) {
  uint32_t keep;
  asm volatile(
      "s_mov_b32 %0, m0\n\ts_mov_b32 m0, %4\n\ts_nop 0\n\tglobal_load_lds_dwordx4 %1, %3 sc1\n\t"
      "global_load_lds_dwordx4 %2, %3 offset:1024 sc1\n\ts_mov_b32 m0, %0"
      : "=&s"(keep)
      : "v"(v0), "v"(v1), "s"(sb), "s"(lds_dst)
      : "memory");
}
__device__ __forceinline__ void glds4(const void* sb0, const void* sb1, uint32_t v0, uint32_t v1, uint32_t v2, uint32_t v3,
                                      uint32_t lds_dst) {
  uint32_t keep;
  asm volatile(
      "s_mov_b32 %0, m0\n\ts_mov_b32 m0, %7\n\ts_nop 0\n\tglobal_load_lds_dwordx4 %1, %5\n\t"
      "global_load_lds_dwordx4 %2, %5 offset:1024\n\tglobal_load_lds_dwordx4 %3, %6 offset:2048\n\t"
      "global_load_lds_dwordx4 %4, %6 offset:3072\n\ts_mov_b32 m0, %0"
      : "=&s"(keep)
      : "v"(v0), "v"(v1), "v"(v2), "v"(v3), "s"(sb0), "s"(sb1), "s"(lds_dst)
      : "memory");
}
__device__ __forceinline__ constexpr uint32_t lds_base(char*) { return 256; }
__device__ __forceinline__ void bar() {
  __builtin_amdgcn_sched_barrier(0);
  asm volatile("" ::: "memory");
  __builtin_amdgcn_s_barrier();
  asm volatile("" ::: "memory");
  __builtin_amdgcn_sched_barrier(0);
}
__device__ __forceinline__ void bar_lds() {
  asm volatile("s_waitcnt lgkmcnt(0)" ::: "memory");
  bar();
}
template <int W> __device__ __forceinline__ void lds_signal(uint32_t U) {
  asm volatile("s_mov_b64 exec, 1\n\tds_add_u32 %0, %0 offset:%1\n\ts_mov_b64 exec, -1" ::"v"(U), "n"(W * 4 - 4) : "memory");
}
template <int W> __device__ __forceinline__ uint32_t lds_peek(uint32_t U) {
  return __builtin_amdgcn_readfirstlane(*(volatile lds_u32*)(size_t)(U + (W * 4 - 4)));
}
template <int W, int SLEEP> __device__ __forceinline__ void lds_wait(uint32_t U, uint32_t n) {
  uint32_t v, c;
  if (SLEEP)
    asm volatile(
        "s_branch 2f\n1:\n\ts_sleep %5\n2:\n\tds_read_b32 %0, %2 offset:%4\n\ts_waitcnt lgkmcnt(0)\n\tv_readfirstlane_b32 %1, %0\n\t"
        "s_cmp_lt_u32 %1, %3\n\ts_cbranch_scc1 1b"
        : "=&v"(v), "=&s"(c)
        : "v"(U), "s"(n * U), "n"(W * 4 - 4), "n"(SLEEP)
        : "memory", "scc");
  else
    asm volatile(
        "1:\n\tds_read_b32 %0, %2 offset:%4\n\ts_waitcnt lgkmcnt(0)\n\tv_readfirstlane_b32 %1, %0\n\t"
        "s_cmp_lt_u32 %1, %3\n\ts_cbranch_scc1 1b"
        : "=&v"(v), "=&s"(c)
        : "v"(U), "s"(n * U), "n"(W * 4 - 4)
        : "memory", "scc");
}
__device__ __forceinline__ float sel_f(uint64_t m, float a, float b) {
  float r;
  asm("v_cndmask_b32_e64 %0, %1, %2, %3" : "=v"(r) : "v"(b), "v"(a), "s"(m));
  return r;
}
__device__ __forceinline__ cf sel_c(uint64_t m, cf a, cf b) { return cf{sel_f(m, a.x, b.x), sel_f(m, a.y, b.y)}; }
__device__ __forceinline__ u64 ld_dev(const u64* p) { return __hip_atomic_load(p, __ATOMIC_RELAXED, __HIP_MEMORY_SCOPE_AGENT); }
__device__ __forceinline__ void st_dev(u64* p, u64 v) { __hip_atomic_store(p, v, __ATOMIC_RELAXED, __HIP_MEMORY_SCOPE_AGENT); }
__device__ __forceinline__ void st_dev(uint32_t* p, uint32_t v) {
  __hip_atomic_store(p, v, __ATOMIC_RELAXED, __HIP_MEMORY_SCOPE_AGENT);
}
__device__ __forceinline__ f32x4 mfma_b(u32x4 a, u32x4 b, f32x4 c) {
  return __builtin_amdgcn_mfma_f32_16x16x32_bf16(__builtin_bit_cast(bf16x8, a), __builtin_bit_cast(bf16x8, b), c, 0, 0, 0);
}
struct PQ {
  u32x4 p, q;
};
constexpr f32x4 Z4 = f32x4{0.f, 0.f, 0.f, 0.f};
constexpr float S16 = 0.283235f;
constexpr unsigned SPIN_MAX = 1u << 20;

constexpr size_t WS_EPOCH = 0;
constexpr size_t WS_F1 = 4096;
constexpr size_t WS_P2 = 8192;
constexpr size_t WS_HF = 1u << 19;
constexpr size_t WS_AGG = 1u << 20;
constexpr size_t WS_ZB = 2u << 20;
constexpr size_t WS_ZC = 8u << 20;
constexpr size_t WS_HS = 18u << 20;
constexpr size_t WS_PS = 26u << 20;

constexpr int NX = 14;
constexpr int G0_STAGE = 32768;
constexpr int L_Z = 0;
constexpr int L_X = 131072;
constexpr int L_TB = L_X + 16384;
constexpr int L_WS = L_TB + 4096;
constexpr int L_PART = L_WS + 4096;
constexpr int L_GRP = L_PART + 3584;
constexpr int L_RS = L_GRP + 2048;
constexpr int L_SYNC = -256;
constexpr int L_PRM = L_RS + 1024;
constexpr int L_SCR2 = L_PRM + 1024;
constexpr int LDS_BYTES = L_SCR2 + 256 + 256;
enum { SW_PART = 1, SW_GRP, SW_PUB, SW_CP, SW_S0, SW_HD, SW_G1, SW_FS, SW_G0E, SW_HS, SW_LD, SW_LD2, SW_F1, SW_PX, SW_FR, SW_PL, SW_P2 };

struct Args {
  const uint32_t* x2;
  const u16 *w_r, *w_i, *xB, *Lre, *Lim, *Bre, *Bim, *Cre, *Cim, *ldt, *Dsk;
  float* out;
  char* ws;
};

struct Tile {
  int tm, tn, nt, late;
};
__device__ __forceinline__ Tile tile_map() {
  const int bid = bid_x(), late = bid >> 7, cq = (bid & 1) * 4 + ((bid >> 5) & 3);
  return Tile{(bid >> 1) & 15, late * 8 + cq, cq, late};
}
__device__ __forceinline__ u32x4 lds4(const void* p) { return *reinterpret_cast<const u32x4*>(p); }
__device__ __forceinline__ u32x4 ldsa(uint32_t a) { return *(__attribute__((address_space(3))) u32x4*)(size_t)a; }
__device__ __forceinline__ void lds_cf2(const void* p, cf& a, cf& b) {
  const f32x4 v = *reinterpret_cast<const f32x4*>(p);
  a = cf{v[0], v[1]};
  b = cf{v[2], v[3]};
}
template <int W> __device__ __forceinline__ void group_sync(uint32_t U, uint32_t n) {
  wait_lgkm0();
  lds_signal<W>(U);
  lds_wait<W, 0>(U, n);
}
__device__ __forceinline__ int k_rot() { return (bid_x() & 6) * 2 + (bid_x() & 1) * 8; }
__device__ __forceinline__ void stage0(const Args& A, char* smem, int wave, int lane, int kt, int parts = 3) {
  const Tile T = tile_map();
  const int prow = lane >> 3;
  const uint32_t va = (T.tm * 128 + wave * 32 + prow) * 2048 + ((lane & 7) ^ (prow & 3)) * 16;
  const uint32_t vb = (T.tn * 64 + wave * 16 + prow) * 2048 + ((lane & 7) ^ prow) * 16;
  const uint32_t dst = lds_base(smem) + wave * 4096 + (kt & 3) * G0_STAGE;
  kt = (kt + k_rot()) & 15;
  if (parts & 1) glds4(A.xB + kt * 64, A.xB + kt * 64, va, va + 15360, (va ^ 64) + 2 * 15360, (va ^ 64) + 3 * 15360, dst);
  if (parts & 2) glds4(A.Bre + kt * 64, A.Bim + kt * 64 - 1024, vb, vb + 15360, vb, vb + 15360, dst + 16384);
}
__device__ __forceinline__ int g1_kt(int late, int s) {
  const int c = 2 * tile_map().nt, g = c & 8;
  if (late && s >= 2 && s < 10) return (g ^ 8) + s - 2;
  if (late && s >= 10) s -= 8;
  if (s >= 8) return (g ^ 24) + s + (late ? -8 : 8 - NX);
  return late * 16 + (s < 2 ? c + s : g + (s - 2 < (c & 7) ? s - 2 : s));
}
__device__ __forceinline__ int g1_rot(int wr) { return 1 ? (tile_map().nt ^ (wr << 1)) & 3 : 0; }
__device__ __forceinline__ uint32_t g1_vc(int wave, int lane) {
  return (tile_map().nt * 128 + (wave >> 1) * 64 + (wave & 1) * 2 + (lane >> 3) * 4) * 2048 + ((lane & 7) ^ (lane >> 3)) * 16;
}
__device__ __forceinline__ void stage_c(const Args& A, int wave, int lane, int s) {
  const int kt = g1_kt(tile_map().late, s);
  const u16* src = ((kt & 1) ? A.Cim : A.Cre) + (kt >> 1) * 64;
  const uint32_t vc = g1_vc(wave, lane);
  glds4(src, src, vc, vc + 65536 - 1024, vc, vc + 65536 - 1024, 256 + (s + 1) * G0_STAGE + 16384 + wave * 4096);
}
__device__ __forceinline__ void warm1(char* smem, const void* base, uint32_t off) {
  uint32_t keep;
  asm volatile("s_mov_b32 %0, m0\n\ts_mov_b32 m0, %3\n\ts_nop 0\n\tglobal_load_lds_dword %1, %2\n\ts_mov_b32 m0, %0"
               : "=&s"(keep)
               : "v"(off), "s"(base), "s"(lds_base(smem) + L_SCR2)
               : "memory");
}
__device__ __forceinline__ void warm_dev(char* smem, const void* base, uint32_t off) {
  uint32_t keep;
  asm volatile("s_mov_b32 %0, m0\n\ts_mov_b32 m0, %3\n\ts_nop 0\n\tglobal_load_lds_dword %1, %2 sc1\n\ts_mov_b32 m0, %0"
               : "=&s"(keep)
               : "v"(off), "s"(base), "s"(lds_base(smem) + L_SCR2)
               : "memory");
}
__device__ __forceinline__ void warm_r(const Args& A, char* smem, int lane, int c, int i) {
  const int bid = bid_x(), tm = ((bid >> 1) & 3) + (c & 3) * 4, nt = (bid & 1) * 4 + (c >> 2);
  uint64_t px = (uint64_t)A.xB, pbr = (uint64_t)A.Bre, pbi = (uint64_t)A.Bim;
  asm volatile("" : "+s"(px), "+s"(pbr), "+s"(pbi));
  px += (tm * 128 + (nt & 3) * 32 + (lane & 31)) * 2048;
  const uint64_t pb = ((lane & 16) ? pbi : pbr) + (nt * 64 + (c & 3) * 16 + (lane & 15)) * 2048;
  const int hi = lane >> 5;
  const bool k2 = i == 4 || i == 6;
  const int sl = i < 3 ? i + 1 : (k2 ? i - 4 : i + 1) + hi;
  const uint64_t pfp = ((k2 || (i < 3 && hi)) ? pb + (k2 ? 1 << 20 : 0) : px) + ((sl + k_rot()) & 15) * 128;
  uint32_t keep;
  asm volatile("s_mov_b32 %0, m0\n\ts_mov_b32 m0, %2\n\ts_nop 0\n\tglobal_load_lds_dword %1, off\n\ts_mov_b32 m0, %0"
               : "=&s"(keep)
               : "v"(pfp), "s"(lds_base(smem) + L_SCR2)
               : "memory");
}
__device__ __forceinline__ u32x2 lds_tr(uint32_t a) {
  return __builtin_bit_cast(u32x2, __builtin_amdgcn_ds_read_tr16_b64_v4i16((__attribute__((address_space(3))) s16x4*)(size_t)a));
}
__device__ __forceinline__ void sta4(uint32_t a, u32x4 v) { *(__attribute__((address_space(3))) u32x4*)(size_t)a = v; }
template <int P> __device__ __forceinline__ void prio(bool on) {
  __builtin_amdgcn_sched_barrier(0);
  if (on) __builtin_amdgcn_s_setprio(P);
  __builtin_amdgcn_sched_barrier(0);
}
__device__ __forceinline__ void row_head(u32x4 (&x)[2][2], const PQ& m16, const PQ& m1, const cf (&tw)[2][4], bool on) {
  f32x4 dr[2][2], di[2][2];
#pragma unroll
  for (int k = 0; k < 2; ++k) {
    const f32x4 ur = mfma_b(x[k][0], m16.p, Z4), ui = mfma_b(x[k][0], m16.q, Z4);
    const u32x4 xn = x[k][1] ^ 0x80008000u;
    dr[k][0] = mfma_b(x[k][1], m1.p, ur), di[k][0] = mfma_b(x[k][1], m1.q, ui);
    dr[k][1] = mfma_b(xn, m1.p, ur), di[k][1] = mfma_b(xn, m1.q, ui);
  }
  prio<0>(on);
#pragma unroll
  for (int i = 0; i < 16; ++i) {
    const int k = i >> 3, t = (i >> 2) & 1, r = i & 3;
    const cf v = fin(cmul(cf{dr[k][t][r], di[k][t][r]}, tw[t][r]));
    dr[k][t][r] = v.x, di[k][t][r] = v.y;
  }
  prio<1>(on);
#pragma unroll
  for (int i = 0; i < 16; ++i) x[i >> 3][(i >> 2) & 1][i & 3] = pk_bf(dr[i >> 3][(i >> 2) & 1][i & 3], di[i >> 3][(i >> 2) & 1][i & 3]);
}
template <int W, bool B, bool F> __device__ __forceinline__ void poll_count(char* ws, int idx, uint32_t n, uint32_t U, uint32_t& c0, uint32_t& c1, uint32_t& c2) {
  uint32_t t, lim = SPIN_MAX;
#define ASK(c) "global_atomic_add " c ", %5, %5, %6 sc0\n\t"
#define CHK(c) "s_waitcnt vmcnt(%10)\n\tv_readfirstlane_b32 %3, " c "\n\ts_cmp_ge_u32 %3, %7\n\ts_cbranch_scc1 2f\n\t" ASK(c)
  asm volatile(
      "s_mov_b64 exec, 1\n\t" ASK("%0") "s_sleep 4\n\t" ASK("%1") "s_sleep 4\n\t" ASK("%2") ".if %11\n\ts_barrier\n.endif\n1:\n\t" CHK("%0") CHK("%1") CHK("%2")
      "s_sub_u32 %4, %4, 1\n\ts_cbranch_scc0 1b\n2:\n\tds_add_u32 %8, %8 offset:%9\n\ts_mov_b64 exec, -1\n.if %12\n\ts_waitcnt vmcnt(0)\n.endif"
      : "=&v"(c0), "=&v"(c1), "=&v"(c2), "=&s"(t), "+s"(lim)
      : "v"(0), "s"(ws + WS_HF + idx * 128), "s"(n), "v"(U), "n"(W * 4 - 4), "n"(2), "n"(B), "n"(F)
      : "memory", "scc");
#undef ASK
#undef CHK
}
template <int W, bool B = false> __device__ __forceinline__ void poll_count(char* ws, int idx, uint32_t n, uint32_t U) {
  uint32_t c0, c1, c2;
  poll_count<W, B, true>(ws, idx, n, U, c0, c1, c2);
}
__device__ __forceinline__ bool fft_role(const Args& A, char* smem, int f, int lane) {
  char* const ws = A.ws;
  const Tile T = tile_map();
  const int bid = bid_x(), li = bid & 127;
  int l15 = lane & 15, q = lane >> 4;
  lds_u32* const sw = (lds_u32*)(smem + L_SYNC);
  const uint32_t U = (uint32_t)(size_t)(sw + 1), ldsb = lds_base(smem);
  const __amdgpu_buffer_rsrc_t rzb = __builtin_amdgcn_make_buffer_rsrc((void*)(ws + WS_ZB), 0, 16 << 20, 0x00020000);
  auto fsync = [&](int n) { group_sync<SW_FS>(U, 8 * n); };
  auto bars = [&](int n) {
#pragma nounroll
    for (int i = 0; i < n; ++i) bar();
  };
  u64 epoch = 0;
  if (f == 0) epoch = ld_dev((const u64*)(ws + WS_EPOCH));
  if (f == 7) {
    const __amdgpu_buffer_rsrc_t ra = __builtin_amdgcn_make_buffer_rsrc((void*)(ws + WS_AGG), 0, 512 << 10, 0x00020000);
    __builtin_amdgcn_raw_buffer_store_b128(u32x4{0u, 0u, 0u, 0u}, ra, (T.tm * 2048 + T.tn * 64 + lane) * 16, 0, 16);
    __builtin_amdgcn_raw_buffer_store_b128(u32x4{0u, 0u, 0u, 0u}, ra, (T.tm * 2048 + 1024 + T.tn * 64 + lane) * 16, 0, 16);
    if (!T.late && lane < (T.tn ? 1 : 4)) st_dev((uint32_t*)(ws + WS_HF + (lane ? 4 * T.tm + lane - 1 : 64 + li) * 128), 0u);
    if (lane == 0) st_dev((uint32_t*)(ws + WS_P2) + bid, 0u);
  }
  __builtin_amdgcn_sched_barrier(0);
  auto ks_of = [&](int slot) {
    const int w = slot & 7, k = bid ? bid + 256 * w : (w < 4 ? 256 * w : 256 * w - 896);
    return slot < 8 ? k : (k ? 2048 - k : 1024);
  };
  {
    const int e = f & 3, n = 4 * q + e;
    uint32_t* tb = reinterpret_cast<uint32_t*>(smem + L_TB) + lane * 4;
    uint32_t* tx = reinterpret_cast<uint32_t*>(smem + L_X) + lane * 4;
    if (f < 4) {
      const cf c = cis<16>(n * l15) * S16;
      tb[e] = pk_bf(c.x, -c.y);
      tb[256 + e] = pk_bf(c.y, c.x);
      if (!T.late) {
        const cf d = cis<32>((2 * n + 1) * l15) * S16;
        tx[e] = pk_bf(d.x, -d.y);
        tx[256 + e] = pk_bf(d.y, d.x);
      }
    }
    if (!T.late) {
      *reinterpret_cast<cf*>(tx + 512 + (f >> 1) * 256 + (f & 1) * 2) = cis<512>(n * (16 * (f >> 2) + l15)) * (1.0f / (64 * S16 * S16));
      const cf w = cis<1024>(l15 + 16 * (f >> 2) + 32 * n);
      *reinterpret_cast<cf*>(tx + 1536 + (f >> 1) * 256 + (f & 1) * 2) = cf{w.y, -w.x};
      const int slot = f < 4 ? l15 : n, u = f < 4 ? q >> 1 : l15 & 1, j = f < 4 ? n & 7 : l15 >> 1;
      const cf g = cis<2048>(j * ks_of(slot)) * (((bid ? slot >> 3 : (slot >> 2) & 1) == u) ? 0.5f : 0.f);
      const uint32_t cj = slot < 8 ? 0u : 0x80000000u;
      tx[2560 + (f >> 2) * 512 + e] = pk_bf(g.x, -g.y) ^ (f < 4 ? 0u : cj);
      tx[2816 + (f >> 2) * 512 + e] = pk_bf(g.y, g.x) ^ (f < 4 ? cj | cj >> 16 : cj);
    }
  }
  if (!T.late) {
    if (f == 0) wait_vm<1>(); else if (f == 7) wait_vm<4>(); else wait_vm<0>();
  }
  bar_lds();
  PQ m16, m1, m8;
  cf tw[2][4];
  u32x4 wv[4];
  m16.p = lds4(smem + L_TB + lane * 16), m16.q = lds4(smem + L_TB + 1024 + lane * 16);
  PQ mk[2];
  auto col_mats = [&](bool b) {
#pragma unroll
    for (int i = 0; i < 8; ++i) {
      const cf c = cis<256>((4 * q + (i & 3)) * (16 * l15 + 2 * f + (i >> 2))) * (1.0f / (16 * S16));
      uint32_t p = pk_bf(c.x, -c.y), o = pk_bf(c.y, c.x);
      asm volatile("" : "+v"(p), "+v"(o));
      mk[i >> 2].p[i & 3] = p, mk[i >> 2].q[i & 3] = o;
      if (b && (i & 1)) bar();
    }
    asm volatile("" : "+v"(mk[0].p), "+v"(mk[0].q), "+v"(mk[1].p), "+v"(mk[1].q));
  };
  auto col256 = [&](uint32_t img, int n, bool cj) {
    typedef __attribute__((address_space(3))) u32x2 lds_u32x2;
    const uint32_t own = img + f * 4096 + q * 512, xi = own + (q & 1) * 64 + l15 * 4;
    u32x4 x[4], d[4];
#pragma unroll
    for (int i = 0; i < 16; ++i) x[i >> 2][i & 3] = *(lds_u32*)(size_t)((xi ^ ((i & 4) * 16)) + (i >> 3) * 2048 + (i & 3) * 128);
    wait_lgkm0();
    lds_signal<SW_FS>(U);
#pragma unroll
    for (int i = 0; i < 4; ++i) {
      const f32x4 re = mfma_b(m16.p, x[i], Z4), im = mfma_b(m16.q, x[i], Z4);
      d[i] = u32x4{pk_bf(re[0], im[0]), pk_bf(re[1], im[1]), pk_bf(re[2], im[2]), pk_bf(re[3], im[3])};
    }
    const uint32_t xo = img + q * 8192 + (f >> 1) * 512 + l15 * 16 + (f & 1) * 8;
    asm volatile("" : "+v"(d[0]), "+v"(d[1]), "+v"(d[2]), "+v"(d[3]));
    lds_wait<SW_FS, 0>(U, 8 * n);
#pragma unroll
    for (int i = 0; i < 8; ++i) *(lds_u32x2*)(size_t)(xo + (i & 3) * 2048 + (i >> 2) * 256) = u32x2{d[i >> 2][i & 3], d[2 + (i >> 2)][i & 3]};
    fsync(n + 1);
#pragma unroll
    for (int i = 0; i < 4; ++i) x[i] = ldsa(own + l15 * 16 + (i >> 1) * 2048 + (i & 1) * 256);
    const uint32_t xt = img + f * 4096 + l15 * 128 + (q ^ (l15 & 7)) * 16;
#pragma unroll
    for (int i = 0; i < 4; ++i) {
      const f32x4 re = mfma_b(x[i], mk[i >> 1].p, Z4);
      f32x4 im = mfma_b(x[i], mk[i >> 1].q, Z4);
      if (cj) im = -im;
      d[i] = u32x4{pk_bf(re[0], im[0]), pk_bf(re[1], im[1]), pk_bf(re[2], im[2]), pk_bf(re[3], im[3])};
    }
    asm volatile("" : "+v"(d[0]), "+v"(d[1]), "+v"(d[2]), "+v"(d[3])::"memory");
#pragma unroll
    for (int i = 0; i < 4; ++i) sta4((xt ^ ((i & 1) * 64)) + (i >> 1) * 2048, d[i]);
    asm volatile("" ::: "memory");
  };
  if (T.late) {
    col_mats(false);
    if (f == 0) wait_vm<1>(); else if (f == 7) wait_vm<3>(); else wait_vm<0>();
    col256(ldsb + 3 * G0_STAGE, 1, false);
    const uint32_t xl = ldsb + 3 * G0_STAGE + f * 4096 + (lane >> 3) * 128 + ((lane ^ (lane >> 3)) & 7) * 16;
#pragma unroll
    for (int i = 0; i < 4; ++i) wv[i] = ldsa(xl + i * 1024);
    const int zo = (2 * f + 16 * (lane >> 3)) * 16384 + (li & 7) * 2048 + (li >> 3) * 128 + (lane & 7) * 16;
    asm volatile("" : "+v"(wv[0]), "+v"(wv[1]), "+v"(wv[2]), "+v"(wv[3]));
#pragma unroll
    for (int i = 0; i < 4; ++i) __builtin_amdgcn_raw_buffer_store_b128(wv[i], rzb, zo + (i >> 1) * 16384 + (i & 1) * (2 << 20), 0, 16);
    bars(2);
  } else {
    const char* tab = smem + L_X + lane * 16;
    m1.p = lds4(tab), m1.q = lds4(tab + 1024), m8.p = lds4(tab + 10240), m8.q = lds4(tab + 11264);
#pragma unroll
    for (int i = 0; i < 4; ++i) lds_cf2(tab + 2048 + i * 1024, tw[i >> 1][(i & 1) * 2], tw[i >> 1][(i & 1) * 2 + 1]);
  }
  wait_vm<0>();
  asm volatile("" : "+v"(epoch));
  const u64 tag = (epoch + 1) | (1ull << 63);
  u64 f1 = tag;
  asm volatile("" : "+v"(f1));
  bar();
  if (f == 0 && lane == 0) st_dev((u64*)(ws + WS_F1) + bid, f1);
  int nb = T.late ? 10 : 11;
  if (T.late && f >= 6) {
    uint64_t pbr = (uint64_t)A.Bre, pbi = (uint64_t)A.Bim;
    asm volatile("" : "+s"(pbr), "+s"(pbi));
    uint64_t pf = ((lane & 16) ? pbi : pbr) + (T.tn * 64 + (T.tm >> 2) * 16 + l15) * 2048 + ((2 * f + (lane >> 5) + k_rot()) & 15) * 128;
    asm volatile("" : "+v"(pf));
    bars(f - 4);
    nb -= f - 4;
    __builtin_amdgcn_s_sleep(5);
    uint32_t keep;
    asm volatile("s_mov_b32 %0, m0\n\ts_mov_b32 m0, %2\n\ts_nop 0\n\tglobal_load_lds_dword %1, off\n\ts_mov_b32 m0, %0"
                 : "=&s"(keep)
                 : "v"(pf), "s"(ldsb + L_SCR2)
                 : "memory");
  }
  cf plam, pcof, lp[11];
  if (f == 2) {
    const u16* raw = reinterpret_cast<const u16*>(smem + L_PRM + 512);
    uint32_t pr = raw[lane], pi = raw[64 + lane], pd = raw[128 + lane];
    asm volatile("" : "+v"(pr), "+v"(pi), "+v"(pd)::"memory");
    auto expf_ = [](float x) {
      const float t = x * 1.44269504088896341f;
      const float r = __builtin_fmaf(x, 1.44269504088896341f, -t) + x * 1.92596299112661746e-8f;
      const float e = __builtin_amdgcn_exp2f(t);
      return __builtin_fmaf(e * r, 0.693147180559945309f, e);
    };
    const float lr = u2f(pr << 16), li = u2f(pi << 16);
    float dt = rbf(expf_(u2f(pd << 16)));
    asm volatile("" : "+v"(dt));
    bar();
    const float zr = dt * lr, zi = dt * li;
    const float e = expf_(zr);
    const float rh = zi * 0.159154936671257019f;
    const float rf = (rh - __builtin_rintf(rh)) + __builtin_fmaf(zi, 6.42063832653e-9f, __builtin_fmaf(zi, 0.159154936671257019f, -rh));
    plam = cf{e * __builtin_amdgcn_cosf(rf), e * __builtin_amdgcn_sinf(rf)};
    asm volatile("" : "+v"(plam));
    bar();
    float den = 2.0f / (lr * lr + li * li);
    asm volatile("" : "+v"(den));
    bar();
    const float nr = plam.x - 1.0f, ni = plam.y;
    pcof = cf{(nr * lr + ni * li) * den, (ni * lr - nr * li) * den};
    lp[0] = cmul(plam, plam);
#pragma unroll
    for (int k = 1; k < 4; ++k) lp[k] = cmul(lp[k - 1], lp[k - 1]);
    asm volatile("" : "+v"(pcof), "+v"(lp[0]), "+v"(lp[1]), "+v"(lp[3]));
    bar();
#pragma unroll
    for (int k = 4; k < 10; ++k) lp[k] = cmul(lp[k - 1], lp[k - 1]);
    lp[10] = cmul(lp[8], lp[9]);
    asm volatile("" : "+v"(lp[4]), "+v"(lp[5]), "+v"(lp[6]), "+v"(lp[8]), "+v"(lp[9]), "+v"(lp[10]));
    nb -= 4;
  }
  bars(nb);
  u64 v[4];
  auto poll1 = [&]() {
#pragma unroll
    for (int i = 0; i < 4; ++i) v[i] = ld_dev((const u64*)(ws + WS_F1) + lane + 64 * i);
  };
  if (f == 0) {
    __builtin_amdgcn_s_sleep(5);
    poll1();
  }
  int zo = (q < 2 ? bid : (bid ? 256 - bid : 128)) * 16384 + (q & 1) * 8192 + f * 256 + l15 * 16;
  asm volatile("" : "+v"(zo));
  bar();
  if (T.late && f >= 4) {
    __builtin_amdgcn_s_sleep(6);
    warm1(smem, (f & 1) ? A.Cre : A.Cim, (T.nt * 32 + T.tm * 2 + ((f >> 1) & 1)) * 8192 + lane * 128);
  }
  if (f == 2) {
    cf* prm = reinterpret_cast<cf*>(smem + L_PRM);
    prm[lane] = plam;
    prm[64 + lane] = pcof;
    cf* pt = reinterpret_cast<cf*>(smem + L_PART) + lane;
    pt[64] = lp[3], pt[128] = lp[4], pt[192] = lp[5], pt[256] = lp[0], pt[320] = lp[1], pt[384] = lp[6];
    pt = reinterpret_cast<cf*>(smem + L_GRP) + lane;
    pt[0] = cf{1.f, 0.f}, pt[64] = lp[8], pt[128] = lp[9], pt[192] = lp[10];
    wait_lgkm0();
    lds_signal<SW_PART>(U);
    lds_signal<SW_LD2>(U);
  }
  if (f == 0) {
    __builtin_amdgcn_s_setprio(2);
#pragma nounroll
    for (unsigned spins = 0; spins < SPIN_MAX; ++spins) {
      bool ok = true;
#pragma unroll
      for (int i = 0; i < 4; ++i) ok &= v[i] == tag;
      if (__builtin_amdgcn_ballot_w64(!ok) == 0) break;
      __builtin_amdgcn_s_sleep(16);
      poll1();
    }
    lds_signal<SW_F1>(U);
    asm volatile("" ::"v"(v[0]), "v"(v[1]), "v"(v[2]), "v"(v[3]));
  }
  u32x4 zi[4];
  if (!T.late) {
    if (f) lds_wait<SW_F1, 1>(U, 1);
#pragma unroll
    for (int e = 0; e < 4; ++e) zi[e] = __builtin_amdgcn_raw_buffer_load_b128(rzb, zo + e * 2048, 0, 16);
  }
  if (f == 0) __builtin_amdgcn_s_setprio(0);
  if (f == 0 && lane == 0) {
    if (T.late) st_dev((uint32_t*)(ws + WS_P2) + bid, 256u);
    if (bid == 0) st_dev((u64*)(ws + WS_EPOCH), epoch + 1);
  }
  if ((bid | f) == 0) __builtin_amdgcn_s_setprio(3);
  if (!T.late) {
    const __amdgpu_buffer_rsrc_t rr = __builtin_amdgcn_make_buffer_rsrc((void*)A.w_r, 0, 2048 * 1026, 0x00020000);
    const __amdgpu_buffer_rsrc_t ri = __builtin_amdgcn_make_buffer_rsrc((void*)A.w_i, 0, 2048 * 1026, 0x00020000);
    uint32_t sr, si;
    auto wload = [&](int ka) {
      const int oa = ka * 1026, ob = ((2048 - ka) & 2047) * 1026;
      wv[0] = __builtin_amdgcn_raw_buffer_load_b128(rr, lane * 16, oa & ~3, 2);
      wv[1] = __builtin_amdgcn_raw_buffer_load_b128(ri, lane * 16, oa & ~3, 2);
      wv[2] = __builtin_amdgcn_raw_buffer_load_b128(rr, lane * 16, (ob + 2) & ~3, 2);
      wv[3] = __builtin_amdgcn_raw_buffer_load_b128(ri, lane * 16, (ob + 2) & ~3, 2);
      const int so = lane == 0 ? ob : (lane == 1 ? oa + 1024 : ((ka & 1) ? oa + 1022 : ob + 1024));
      sr = __builtin_amdgcn_raw_buffer_load_b16(rr, so, 0, 2);
      si = __builtin_amdgcn_raw_buffer_load_b16(ri, so, 0, 2);
    };
    const bool two = (bid | f) == 0;
    u32x4 w2r, w2i;
    uint32_t s2r, s2i;
    if (two) {
      wload(1024);
      w2r = wv[0], w2i = wv[1], s2r = sr, s2i = si;
    }
    wload(ks_of(f));
    __builtin_amdgcn_sched_barrier(0);
    {
      u32x4 o[4];
      asm volatile("" : "+v"(zi[0]), "+v"(zi[1]), "+v"(zi[2]), "+v"(zi[3]));
#pragma unroll
      for (int c = 0; c < 4; ++c) {
        const u32x4 a = u32x4{zi[0][c], zi[1][c], zi[2][c], zi[3][c]};
        const f32x4 re = mfma_b(a, m8.p, Z4), im = mfma_b(a, m8.q, Z4);
#pragma unroll
        for (int r = 0; r < 4; ++r) o[r][c] = pk_bf(re[r], im[r]);
      }
      const uint32_t oa = l15 * 2048 + f * 256 + ((4 * q) ^ (l15 & 7) ^ ((f & 2) << 1)) * 16;
#pragma unroll
      for (int r = 0; r < 4; ++r) sta4(ldsb + L_Z + (oa ^ (r * 16)), o[r]);
      wait_lgkm0();
      lds_signal<SW_FS>(U);
    }
    auto shift = [](u32x4& w, uint32_t last) {
      const uint32_t nx = (uint32_t)__builtin_amdgcn_update_dpp((int)last, (int)(uint32_t)w[0], 0x130, 0xF, 0xF, false);
      w = u32x4{__builtin_amdgcn_alignbit(w[1], w[0], 16), __builtin_amdgcn_alignbit(w[2], w[1], 16),
                __builtin_amdgcn_alignbit(w[3], w[2], 16), __builtin_amdgcn_alignbit(nx, w[3], 16)};
    };
    uint32_t wsp = 0;
    if (two) {
      sta4(ldsb + L_WS + lane * 16, w2r), sta4(ldsb + L_WS + 1024 + lane * 16, w2i);
      shift(w2r, s2r), shift(w2i, s2i);
      sta4(ldsb + L_WS + 2048 + lane * 16, w2r), sta4(ldsb + L_WS + 3072 + lane * 16, w2i);
      wsp = s2r | s2i << 16;
    }
    asm volatile("" : "+v"(wsp));
    lds_wait<SW_FS, 0>(U, 8);
    prio<1>(!two);
    const uint32_t row = ldsb + L_Z + f * 2048;
    u32x4 x[2][2];
    auto rows = [&]() {
      const uint32_t ra = row + q * 512 + ((l15 >> 2) ^ f ^ ((q & 1) << 2)) * 16 + (l15 & 3) * 4;
#pragma unroll
      for (int i = 0; i < 16; ++i) x[i >> 3][(i >> 2) & 1][i & 3] = *(lds_u32*)(size_t)((ra ^ ((i & 4) * 16)) + (i & 3) * 128 + (i >> 3) * 16384);
    };
    rows();
    if (two) x[1][0] = x[0][0] ^ 0x80000000u, x[1][1] = x[0][1] ^ 0x80000000u;
    uint32_t wa = row, wb = two ? ldsb + L_X + 14336 : row + 16384;
    asm volatile("" : "+v"(x[0][0]), "+v"(x[0][1]), "+v"(x[1][0]), "+v"(x[1][1])::"memory");
    row_head(x, m16, m1, tw, !two);
    if (bid & 1) shift(wv[0], sr), shift(wv[1], si);
    else shift(wv[2], sr), shift(wv[3], si);
    wsp = (uint32_t)__builtin_amdgcn_update_dpp((int)(sr | si << 16), (int)wsp, 0x114, 0xF, 0x2, false);
    sta4(wa + lane * 16, wv[0]);
    sta4(wa + 1024 + lane * 16, wv[1]);
    sta4(wb + lane * 16, wv[2]);
    sta4(wb + 1024 + lane * 16, wv[3]);
    asm volatile("" : "+v"(wsp)::"memory");
#pragma nounroll
    for (int jb = 0;; jb = 4) {
      u32x2 fw[4][2];
#pragma unroll
      for (int h = 0; h < 2; ++h) {
        fw[0][h] = lds_tr(wa + q * 256 + l15 * 8 + h * 128);
        fw[1][h] = lds_tr(wa + q * 256 + l15 * 8 + h * 128 + 1024);
        fw[2][h] = lds_tr(wb + (3 - q) * 256 + l15 * 8 + h * 128);
        fw[3][h] = lds_tr(wb + (3 - q) * 256 + l15 * 8 + h * 128 + 1024);
      }
#pragma unroll
      for (int i = 0; i < 8; ++i)
        fw[2 + (i >> 2)][(i >> 1) & 1][i & 1] =
            (uint32_t)__builtin_amdgcn_update_dpp(0, (int)(uint32_t)fw[2 + (i >> 2)][(i >> 1) & 1][i & 1], 0x140, 0xF, 0xF, true);
      const uint32_t s0 = __builtin_amdgcn_readlane(wsp, jb), s1 = __builtin_amdgcn_readlane(wsp, jb + 1);
#pragma unroll
      for (int t = 0; t < 2; ++t) {
        cf u[4];
        lds_cf2(smem + L_X + 6144 + t * 2048 + lane * 16, u[0], u[1]), lds_cf2(smem + L_X + 7168 + t * 2048 + lane * 16, u[2], u[3]);
        f32x4 ar = mfma_b(m16.p, x[0][t], Z4), ai = mfma_b(m16.q, x[0][t], Z4), br = mfma_b(m16.p, x[1][t], Z4), bi = mfma_b(m16.q, x[1][t], Z4);
        auto half = [](uint32_t w, int hi) { return u2f(hi ? w & 0xFFFF0000u : w << 16); };
        cf lo[4];
#pragma unroll
        for (int r = 0; r < 4; ++r) {
          const int rr = t ? 3 - r : r;
          lo[r] = fin(cf{half(fw[2 * t][rr >> 1][rr & 1], 0), half(fw[2 * t + 1][rr >> 1][rr & 1], 0)});
        }
        prio<0>(!two);
#pragma unroll
        for (int r = 0; r < 4; ++r) {
          const int rr = t ? r : 3 - r;
          const cf hi = cf{half(fw[2 - 2 * t][rr >> 1][rr & 1], 1), half(fw[3 - 2 * t][rr >> 1][rr & 1], 1)};
          cf fa = t ? hi : lo[r], fb = t ? lo[r] : hi;
          fb.y = -fb.y;
          if (t == 0 && r == 0 && lane == 0) {
            fa = (fa + cf{half(s0, 0), -half(s0, 1)}) * 0.5f;
            fb = (fb + cf{half(s1, 0), half(s1, 1)}) * 0.5f;
          }
          const cf E = cf{ar[r] + br[r], ai[r] + bi[r]}, D = cf{ar[r] - br[r], ai[r] - bi[r]};
          const cf X1 = cmad(u[r], D, E), X2 = cf{__builtin_fmaf(2.f, E.x, -X1.x), __builtin_fmaf(2.f, E.y, -X1.y)};
          const cf Yb = cmul(X2, fb), S = cmad(X1, fa, Yb);
          const cf Df = cf{__builtin_fmaf(-2.f, Yb.x, S.x), __builtin_fmaf(-2.f, Yb.y, S.y)};
          const cf O1 = fin(cmad(cf{u[r].x, -u[r].y}, Df, S));
          const cf O2 = fin(cf{__builtin_fmaf(2.f, S.x, -O1.x), __builtin_fmaf(-2.f, S.y, O1.y)});
          ar[r] = O1.x, ai[r] = O1.y, br[r] = O2.x, bi[r] = O2.y;
        }
        prio<1>(!two);
#pragma unroll
        for (int r = 0; r < 4; ++r) x[0][t][r] = pk_bf(ar[r], -ai[r]), x[1][t][r] = pk_bf(br[r], bi[r]);
      }
      if (!two) break;
      if (jb) {
        x[0][0] = ldsa(row + lane * 16), x[0][1] = ldsa(row + 1024 + lane * 16);
        break;
      }
      sta4(row + lane * 16, x[0][0]), sta4(row + 1024 + lane * 16, x[0][1]);
      rows();
      x[0][0] = x[1][0] ^ 0x80000000u, x[0][1] = x[1][1] ^ 0x80000000u;
      row_head(x, m16, m1, tw, false);
      wa = ldsb + L_WS, wb = ldsb + L_WS + 2048;
    }
    row_head(x, m16, m1, tw, !two);
    const uint32_t oa = row + l15 * 128 + (q ^ (l15 & 7)) * 16;
#pragma unroll
    for (int i = 0; i < 4; ++i) {
      const f32x4 re = mfma_b(x[i >> 1][i & 1], m16.p, Z4), im = mfma_b(x[i >> 1][i & 1], m16.q, Z4);
      sta4((oa ^ ((i & 1) * 64)) + (i >> 1) * 16384, u32x4{pk_bf(re[0], im[0]), pk_bf(re[1], im[1]), pk_bf(re[2], im[2]), pk_bf(re[3], im[3])});
    }
    m8.p = lds4(smem + L_X + 12288 + lane * 16), m8.q = lds4(smem + L_X + 13312 + lane * 16);
    fsync(2);
    u32x4 yi[4];
#pragma unroll
    for (int e = 0; e < 4; ++e) yi[e] = lds4(smem + L_Z + (4 * q + e) * 2048 + f * 256 + (l15 ^ ((2 * f + (l15 >> 3)) & 7)) * 16);
    asm volatile("" : "+v"(yi[0]), "+v"(yi[1]), "+v"(yi[2]), "+v"(yi[3]));
    u32x4 o[4];
#pragma unroll
    for (int c = 0; c < 4; ++c) {
      const u32x4 y = u32x4{yi[0][c], yi[1][c], yi[2][c], yi[3][c]};
      const f32x4 re = mfma_b(m8.p, y, Z4), im = mfma_b(m8.q, y, Z4);
#pragma unroll
      for (int r = 0; r < 4; ++r) o[r][c] = pk_bf(re[r], im[r]);
    }
    const int zc = (int)(WS_ZC - WS_ZB) + q * 4096 + f * 256 + l15 * 16;
    asm volatile("" : "+v"(o[0]), "+v"(o[1]), "+v"(o[2]), "+v"(o[3]));
    __builtin_amdgcn_s_setprio(0);
    nb = 1 + (f >> 2);
    bars(nb);
    if (nb) __builtin_amdgcn_s_sleep(4);
#pragma unroll
    for (int i = 0; i < 4; ++i)
      __builtin_amdgcn_raw_buffer_store_b128(o[i], rzb, zc + ((i & 1) ? (bid ? 256 - bid : 128) : bid) * 16384 + (i >> 1) * 2048, 0, 16);
  } else {
    if (f >= 4) return true;
    nb = 0;
    if (f == 1) {
      lds_wait<SW_PUB, 4>(U, 4);
      lds_wait<SW_F1, 4>(U, 1);
      __builtin_amdgcn_s_sleep(10);
      poll_count<SW_G1>(ws, 4 * T.tm + 1 - (T.nt >> 2), 16, U);
    }
  }
  const int N = T.late ? 32 - NX : NX;
  if (T.late && f != 1) return false;
  static_assert(4 >= 4 && 1 <= 2 && 1 + (7 >> 2) <= 4 && NX - 5 - 4 - 1 >= 0, "barriers");
  if (f != 1) {
    bars(4 - nb);
    wait_vm<0>();
    bar();
  }
  if (f == 1) {
    bars(T.late ? 5 : 3 - nb);
    {
      uint32_t c;
      asm volatile("s_mov_b64 exec, 1\n\tglobal_atomic_add %0, %1, %1, %2 sc0\n\ts_mov_b64 exec, -1\n\ts_barrier\n\ts_waitcnt vmcnt(0)"
                   : "=&v"(c)
                   : "v"(0), "s"(ws + WS_HF + (4 * T.tm + 2) * 128)
                   : "memory");
      if (__builtin_amdgcn_readfirstlane(c) < 32u) poll_count<SW_FR>(ws, 4 * T.tm + 2, 32, U);
    }
    if (T.late) {
      bars(N - 11);
      poll_count<SW_PX, true>(ws, 64 + li, 4, U);
      return false;
    }
    bars(NX - 8);
  } else if (f == 0) {
    if (lane == 0) st_dev((uint32_t*)(ws + WS_P2) + bid, 256u);
    bars(NX - 5 - 4 - 1);
    const __amdgpu_buffer_rsrc_t rp = __builtin_amdgcn_make_buffer_rsrc((void*)(ws + WS_P2), 0, 1024, 0x00020000);
    u32x4 pw = __builtin_amdgcn_raw_buffer_load_b128(rp, lane * 16, 0, 16);
    bars(1);
#pragma nounroll
    for (unsigned spins = 0; spins < SPIN_MAX; ++spins) {
      if (__builtin_amdgcn_ballot_w64((pw[0] & pw[1] & pw[2] & pw[3] & 256u) == 0) == 0) break;
      __builtin_amdgcn_s_sleep(8);
      pw = __builtin_amdgcn_raw_buffer_load_b128(rp, lane * 16, 0, 16);
      asm volatile("" ::: "memory");
    }
    lds_signal<SW_P2>(U);
    asm volatile("" ::"v"(pw));
    if (lane < 2) st_dev((u64*)(ws + WS_F1) + bid + lane * 128, 0ull);
  } else {
    bars(NX - 5 - 4);
  }
  {
    asm volatile("" : "+v"(lane));
    l15 = lane & 15;
    q = lane >> 4;
    constexpr uint32_t img = 256;
    const uint32_t vo = (2 * f + 16 * (lane >> 3)) * 16384 + (li & 7) * 2048 + (li >> 3) * 128 + ((lane & 7) ^ ((lane >> 5) * 4)) * 16;
    if (f) lds_wait<SW_P2, 1>(U, 1);
    if (1 == 1) lds_wait<SW_LD, 1>(U, 8);
    if (1 == 2) lds_wait<SW_LD2, 1>(U, 9);
    gdev2(ws + WS_ZC, vo, vo + (2 << 20) - 1024, img + f * 4096);
    gdev2(ws + WS_ZC + 16384, vo, vo + (2 << 20) - 1024, img + f * 4096 + 2048);
    col_mats(false);
    wait_vm<0>();
    col256(img, 3, true);
    typedef __attribute__((address_space(3))) u32x2 lds_u32x2;
    const uint32_t xr = img + f * 4096 + q * 128 + ((l15 >> 1) ^ q) * 16 + (l15 & 1) * 8;
    u32x2 z[8];
#pragma unroll
    for (int i = 0; i < 8; ++i) z[i] = *(lds_u32x2*)(size_t)((xr ^ ((i & 1) * 64)) + i * 512);
    const __amdgpu_buffer_rsrc_t ro = __builtin_amdgcn_make_buffer_rsrc((void*)A.out, 0, 16 << 20, 0x00020000);
    const int oo = (2 * f + 16 * q) * 65536 + (li & 7) * 8192 + (li >> 3) * 256 + l15 * 16;
    lds_wait<SW_PL, 2>(U, 4);
#pragma unroll
    for (int i = 0; i < 8; ++i) {
      const uint32_t p0 = z[i][0], p1 = z[i][1];
      __builtin_amdgcn_raw_buffer_store_b128(u32x4{p0 << 16, p0 & 0xFFFF0000u, p1 << 16, p1 & 0xFFFF0000u}, ro, oo + (i >> 2) * 65536 + (i & 3) * (4 << 20), 0, 16);
    }
  }
  return false;
}

__device__ __forceinline__ void publish(char* ws, int idx, int lane) {
  wait_vm<0>();
  if (lane == 0) __hip_atomic_fetch_add((uint32_t*)(ws + WS_HF + idx * 128), 1u, __ATOMIC_RELAXED, __HIP_MEMORY_SCOPE_AGENT);
}
__device__ __forceinline__ void loader_role(const Args& A, char* smem, int wave, int lane) {
  char* const ws = A.ws;
  const Tile T = tile_map();
  const int tm = T.tm, tn = T.tn;
  lds_u32* const sw = (lds_u32*)(smem + L_SYNC);
  const uint32_t U = (uint32_t)(size_t)(sw + 1);
  const uint32_t ldsb = lds_base(smem), lds0 = ldsb + wave * 4096;
  const int prow = lane >> 3;
  {
    __builtin_amdgcn_s_setprio(0);
    const uint32_t va = (tm * 128 + wave * 32 + prow) * 2048 + ((lane & 7) ^ (prow & 3)) * 16;
    const uint32_t vb = (tn * 64 + wave * 16 + prow) * 2048 + ((lane & 7) ^ prow) * 16;
    if (T.late) {
      bar();
      const int wc = (bid_x() >> 3) & 15;
      if ((wc & 7) != 3) {
#pragma nounroll
        for (int i = 2 + wave; i < 7; i += 4) warm_r(A, smem, lane, wc, i);
      }
      stage0(A, smem, wave, lane, 0);
    }
    stage0(A, smem, wave, lane, 1);
#pragma nounroll
    for (int k = 2; k < 16; ++k) {
      const uint32_t dst = lds0 + (k & 3) * G0_STAGE;
      const int ko = ((k + k_rot()) & 15) * 64;
      asm volatile(
          "s_mov_b32 m0, %9\n\ts_waitcnt vmcnt(8)\n\ts_barrier\n\t"
          "global_load_lds_dwordx4 %0, %6\n\tglobal_load_lds_dwordx4 %1, %6 offset:1024\n\t"
          "global_load_lds_dwordx4 %2, %6 offset:2048\n\tglobal_load_lds_dwordx4 %3, %6 offset:3072\n\t"
          "s_mov_b32 m0, %10\n\ts_nop 0\n\t"
          "global_load_lds_dwordx4 %4, %7\n\tglobal_load_lds_dwordx4 %5, %7 offset:1024\n\t"
          "global_load_lds_dwordx4 %4, %8 offset:2048\n\tglobal_load_lds_dwordx4 %5, %8 offset:3072"
          ::"v"(va), "v"(va + 15360), "v"((va ^ 64) + 2 * 15360), "v"((va ^ 64) + 3 * 15360), "v"(vb), "v"(vb + 15360),
            "s"(A.xB + ko), "s"(A.Bre + ko), "s"(A.Bim + ko - 1024), "s"(dst), "s"(dst + 16384)
          : "memory");
    }
    wait_vm<8>();
    lds_signal<SW_LD>(U);
    wait_vm<0>();
    lds_signal<SW_LD2>(U);
  }
  __builtin_amdgcn_s_setprio(2);
  {
    const int nseg = 2 * tm - 8 * wave;
    cf G = cf{0.f, 0.f};
    lds_wait<SW_PART, 2>(U, 1);
    lds_wait<SW_F1, 2>(U, 1);
    if (nseg > 0) {
      const __amdgpu_buffer_rsrc_t ra = __builtin_amdgcn_make_buffer_rsrc((void*)(ws + WS_AGG), 0, 512 << 10, 0x00020000);
      int o[8];
#pragma unroll
      for (int k = 0; k < 8; ++k) {
        o[k] = ((nseg - 1 - k) * 1024 + tn * 64 + lane) * 16;
        asm volatile("" : "+v"(o[k]));
      }
      u32x4 u[8];
      cf l64 = reinterpret_cast<const cf*>(smem + L_PART)[192 + lane], l128 = reinterpret_cast<const cf*>(smem + L_PART)[384 + lane];
      cf lw = reinterpret_cast<const cf*>(smem + L_GRP)[wave * 64 + lane];
      asm volatile("" : "+v"(l64), "+v"(l128), "+v"(lw));
      lds_wait<SW_PUB, 1>(U, 4);
      __builtin_amdgcn_s_sleep(10);
#pragma nounroll
      for (unsigned spins = 0; spins < SPIN_MAX; ++spins) {
        uint32_t n = 0;
#pragma unroll
        for (int k = 0; k < 8; ++k) u[k] = __builtin_amdgcn_raw_buffer_load_b128(ra, o[k], 0, 16);
#pragma unroll
        for (int k = 0; k < 8; ++k) n += u[k][1] + u[k][3];
        if (__builtin_amdgcn_ballot_w64(n != 2u * (nseg < 8 ? nseg : 8)) == 0) break;
        asm volatile("" ::: "memory");
      }
#pragma unroll
      for (int k = 6; k >= 0; k -= 2)
        G = cmad(G, l128, cmad(cf{u2f(u[k + 1][0]), u2f(u[k + 1][2])}, l64, cf{u2f(u[k][0]), u2f(u[k][2])}));
      G = cmul(G, lw);
    }
    reinterpret_cast<cf*>(smem + L_GRP)[wave * 64 + lane] = G;
    wait_lgkm0();
    lds_signal<SW_GRP>(U);
  }
  uint32_t c0 = 0, c1 = 0, c2 = 0;
  if (!T.late) {
    lds_wait<SW_GRP, 2>(U, 4);
    lds_wait<SW_G0E, 2>(U, 4);
    stage_c(A, wave, lane, 0);
    if (wave == 0) {
      stage_c(A, wave, lane, 1);
      wait_vm<0>();
      lds_signal<SW_CP>(U);
    }
    lds_wait<SW_HS, 2>(U, 4);
    if (wave == 0) {
      __builtin_amdgcn_s_sleep(12);
      poll_count<SW_G1, false, false>(ws, 4 * tm + (T.nt >> 2), 16, U, c0, c1, c2);
    } else {
      stage_c(A, wave, lane, 1);
    }
    stage_c(A, wave, lane, 2);
    if (wave) {
      wait_vm<4>();
      lds_signal<SW_CP>(U);
    }
  }
  lds_wait<SW_G1, 1>(U, 1);
  __builtin_amdgcn_s_setprio(0);
  const int N = T.late ? 32 - NX : NX;
  const char* ab = ws + WS_HS - 2048 + (size_t)tm * (64 * 8192);
  const uint32_t vh2 = ((wave >> 1) * 262144 + (wave & 1) * 4096 + 2048 + lane * 16) ^ (g1_rot(wave >> 1) * 2048), vh = (vh2 ^ 2048) + 2048;
  const uint32_t vc = g1_vc(wave, lane);
  {
    const char* pa = ab + g1_kt(T.late, 2) * 8192;
    glds2_dev(pa, vh, lds0 + 3 * G0_STAGE);
    glds2_dev(pa + 2048, vh2, lds0 + 3 * G0_STAGE + 2048);
  }
  lds_wait<SW_S0, 1>(U, 4);
  uint32_t dst = lds0 + 3 * G0_STAGE;
#pragma nounroll
  for (int k = 3; k < N; ++k) {
    dst = dst == lds0 + 3 * G0_STAGE ? lds0 + G0_STAGE : dst + G0_STAGE;
    const int kt = g1_kt(T.late, k);
    asm volatile(
        "s_mov_b32 m0, %5\n\ts_cmp_eq_u32 %8, 3\n\ts_cbranch_scc1 1f\n\ts_waitcnt vmcnt(8)\n\ts_barrier\n1:\n\t"
        "global_load_lds_dwordx4 %2, %4 sc1\n\tglobal_load_lds_dwordx4 %2, %4 offset:1024 sc1\n\t"
        "global_load_lds_dwordx4 %7, %4 offset:2048 sc1\n\tglobal_load_lds_dwordx4 %7, %4 offset:3072 sc1\n\t"
        "s_mov_b32 m0, %6\n\ts_nop 0\n\t"
        "global_load_lds_dwordx4 %0, %3\n\tglobal_load_lds_dwordx4 %1, %3 offset:1024\n\t"
        "global_load_lds_dwordx4 %0, %3 offset:2048\n\tglobal_load_lds_dwordx4 %1, %3 offset:3072"
        ::"v"(vc), "v"(vc + 65536 - 1024), "v"(vh), "s"(((kt & 1) ? A.Cim : A.Cre) + (kt >> 1) * 64), "s"(ab + kt * 8192), "s"(dst),
          "s"(dst + 16384), "v"(vh2), "s"(k)
        : "memory", "scc");
  }
  wait_vm<8>();
  asm volatile("" ::"v"(c0), "v"(c1), "v"(c2));
  lds_signal<SW_LD>(U);
  wait_vm<0>();
  lds_signal<SW_LD2>(U);
  if (T.late) {
    const char* ps = ws + WS_PS + (size_t)(bid_x() & 127) * 32768 + wave * 8192;
    lds_wait<SW_PX, 1>(U, 1);
#pragma nounroll
    for (int i = 0; i < 4; ++i) glds2_dev(ps + i * 2048, lane * 16, ldsb + wave * 8192 + i * 2048);
    wait_vm<0>();
    lds_signal<SW_PL>(U);
  }
}

template <bool TR> struct Core {
  struct AF {
    u32x2 l, h;
  };
  f32x4 (&acc)[4][4];
  uint32_t pa0, pa1, pb0, pb1;
  AF a[3][2];
  int js = 0;
  u32x4 b[4][2];
  static constexpr int DA = TR ? 2048 : 512;
  static constexpr int pn(int i) { return (i >> 3) == 1 ? 3 - ((i >> 1) & 3) : (i >> 1) & 3; }
  static constexpr int pk(int i) { return (i ^ (i >> 1)) & 1; }
  __device__ __forceinline__ void ra(int m, int k) {
    AF& d = a[m == 3 ? 1 : m][k];
    if (TR) {
      d.l = lds_tr(pa0 + m * DA + k * 512), d.h = lds_tr(pa1 + m * DA + k * 512);
    } else {
      const u32x4 v = ldsa((k ? pa1 : pa0) + m * DA);
      d.l = u32x2{v[0], v[1]}, d.h = u32x2{v[2], v[3]};
    }
    __builtin_amdgcn_sched_barrier(0);
  }
  __device__ __forceinline__ void rb(int i) {
    b[pn(i)][pk(i)] = ldsa((pk(i) ? pb1 : pb0) + pn(i) * 2048);
    __builtin_amdgcn_sched_barrier(0);
  }
  __device__ __forceinline__ void mm(int i) {
    const AF& x = a[i >= 24 ? 1 : i >> 3][pk(i)];
    const u32x4 av = u32x4{x.l[0], x.l[1], x.h[0], x.h[1]};
    asm volatile("v_mfma_f32_16x16x32_bf16 %0, %1, %2, %0" : "+v"(acc[i >> 3][pn(i)]) : "v"(av), "v"(b[pn(i)][pk(i)]));
    __builtin_amdgcn_sched_barrier(0);
    if (i < 2) ra(1, i);
    if (i == 8) ra(2, 0);
    if (i == 10) ra(2, 1);
    if (i == 15 || i == 16) ra(3, i - 15);
  }
  static __device__ __forceinline__ void add(uint32_t& p, int adv) {
    p += adv;
    asm volatile("" : "+v"(p));
    __builtin_amdgcn_sched_barrier(0);
  }
  template <bool BAR> __device__ __forceinline__ void step(int& k, int k1, uint32_t cnt) {
    int adv;
#pragma unroll
    for (int i = 0; i < 32; ++i) {
      mm(i);
      if (i == 11) {
        adv = TR ? js : k & 3;
        k += 1;
        if (TR) js = js == 2 ? 0 : js + 1;
        asm volatile("" : "+s"(k), "+s"(adv));
        if (TR) asm volatile("" : "+s"(js));
        __builtin_amdgcn_sched_barrier(0);
      }
      if (i == 12) {
        adv = adv == (TR ? 2 : 3) ? (TR ? -2 : -3) * G0_STAGE : G0_STAGE;
        asm volatile("" : "+s"(adv));
        __builtin_amdgcn_sched_barrier(0);
      }
      if (i == 13) add(pb0, adv);
      if (i == 14) add(pb1, adv);
      if (i == 17) add(pa0, adv);
      if (i == 18) add(pa1, adv);
      if (i == 23 && BAR && !TR) asm volatile("s_waitcnt lgkmcnt(0)\n\ts_barrier" ::: "memory");
      if (i == 23 && BAR && TR) {
        asm volatile(
            "s_waitcnt lgkmcnt(0)\n\ts_cmp_eq_u32 %1, 1\n\ts_cbranch_scc1 1f\n\ts_barrier\n\ts_branch 2f\n1:\n\tv_mov_b32 %0, 4\n\ts_mov_b64 exec, 1\n\t"
            "ds_add_u32 %0, %0 offset:%2\n\ts_mov_b64 exec, -1\n2:"
            : "=&v"(a[0][0].h[0])
            : "s"(k), "n"(SW_S0 * 4 - 4)
            : "scc", "memory");
        __builtin_amdgcn_sched_barrier(0);
      }
      if (i == 23 && !BAR) {
        uint32_t c, t;
        asm volatile(
            "s_sub_u32 %3, %4, %5\n\ts_lshl_b32 %3, %3, 2\n\tv_mov_b32 %1, %3\n\ts_add_u32 %3, %3, %6\n1:\n\tds_read_b32 %0, %1 offset:%7\n\t"
            "s_waitcnt lgkmcnt(0)\n\tv_readfirstlane_b32 %2, %0\n\ts_cmp_lt_u32 %2, %3\n\ts_cbranch_scc1 1b"
            : "=&v"(a[0][0].l[0]), "=&v"(a[0][0].l[1]), "=&s"(c), "=&s"(t)
            : "s"(k), "s"(k1), "s"(cnt), "n"(SW_LD * 4)
            : "scc", "memory");
        __builtin_amdgcn_sched_barrier(0);
      }
      if (i == 23) ra(0, 0), ra(0, 1);
      if (i >= 24) rb(i);
    }
  }
  __device__ __forceinline__ void run(int k, int n, uint32_t cnt) {
    const int k1 = k + n - 3;
    if (!TR) bar_lds();
    ra(0, 0), rb(0), ra(0, 1);
#pragma unroll
    for (int i = 1; i < 8; ++i) rb(i);
#pragma nounroll
    do step<true>(k, 0, 0); while (k != k1);
#pragma nounroll
    do step<false>(k, k1 + 1, cnt); while (k != k1 + 2);
  }
};

__device__ __forceinline__ void mfma_role(const Args& A, char* smem, int wave, int lane) {
  char* const ws = A.ws;
  const Tile T = tile_map();
  const int tm = T.tm, tn = T.tn;
  lds_u32* const sw = (lds_u32*)(smem + L_SYNC);
  const uint32_t U = (uint32_t)(size_t)(sw + 1);
  const int wr = wave >> 1, wc = wave & 1;
  int fr = lane & 15, fq = lane >> 4;
  {
    if (T.late) bar_lds();
    if (wave == 3) wait_vm<0>();
    __builtin_amdgcn_s_setprio(1);
  }
  {
    f32x4 acc[4][4];
#pragma unroll
    for (int m = 0; m < 4; ++m)
#pragma unroll
      for (int n = 0; n < 4; ++n) acc[m][n] = Z4;
    const cf* prm = reinterpret_cast<const cf*>(smem + L_PRM) + wc * 32;
    cf* part = reinterpret_cast<cf*>(smem + L_PART) + wc * 32;
    const cf* grp = reinterpret_cast<const cf*>(smem + L_GRP);
    cf lam[2], l16[2], l32[2], s[2][16];
    auto scan = [&](int i, int jp) {
      const int m = i >> 2;
      if ((i & 3) == 0 || i == 1) asm volatile("" : "+v"(acc[m][2 * jp]), "+v"(acc[m][2 * jp + 1]));
      if (i == 1) s[jp][0] = cf{acc[0][2 * jp][0], acc[0][2 * jp + 1][0]};
      float x = acc[m][2 * jp][i & 3], y = acc[m][2 * jp + 1][i & 3];
      asm volatile("v_fma_f32 %0, -%3, %5, %0\n\tv_fmac_f32 %1, %3, %4\n\tv_fmac_f32 %0, %2, %4\n\tv_fmac_f32 %1, %2, %5"
                   : "+v"(x), "+v"(y)
                   : "v"(s[jp][i - 1].x), "v"(s[jp][i - 1].y), "v"(lam[jp].x), "v"(lam[jp].y));
      s[jp][i] = cf{x, y};
      __builtin_amdgcn_sched_barrier(0);
    };
    {
      Core<false> g{acc};
      g.pa0 = lds_base(smem) + (wr * 64 + (fr >> 2) * 16 + (fr & 3)) * 128 + (fq ^ (fr & 7)) * 16;
      g.pb0 = lds_base(smem) + (128 + wc * 64 + fr) * 128 + (fq ^ (fr & 7)) * 16;
      g.pa1 = g.pa0 ^ 64, g.pb1 = g.pb0 ^ 64;
#pragma unroll
      for (int m = 0; m < 4; ++m) asm volatile("" : "+v"(acc[m][0]), "+v"(acc[m][1]), "+v"(acc[m][2]), "+v"(acc[m][3]));
      g.run(0, 16, 16);
#pragma unroll
      for (int i = 0; i < 32; ++i) {
        g.mm(i);
        if (i == 1) {
          asm volatile("" : "+v"(lane));
          lam[0] = prm[lane & 15], lam[1] = prm[16 + (lane & 15)];
          __builtin_amdgcn_s_setprio(3);
          __builtin_amdgcn_sched_barrier(0);
        }
        if (i >= 8 && i < 14) scan(1 + (i - 8) % 3, (i - 8) / 3);
        if (i >= 14 && i < 22) scan(4 + ((i - 14) & 3), i < 18);
        if (i >= 22) scan(8 + ((i - 22) & 3) + (i >= 30 ? 4 : 0), i >= 26 && i < 30);
        if (i == 25) {
          lds_signal<SW_G0E>(U);
          asm volatile("" : "+v"(lane));
          fr = lane & 15;
          fq = lane >> 4;
#pragma unroll
          for (int jp = 0; jp < 2; ++jp) l16[jp] = part[64 + jp * 16 + fr], l32[jp] = part[128 + jp * 16 + fr];
          __builtin_amdgcn_sched_barrier(0);
        }
      }
      scan(14, 0), scan(15, 0);
#pragma unroll
      for (int i = 12; i < 16; ++i) scan(i, 1);
    }
    const uint64_t m1 = 0xFFFF0000FFFF0000ull, m2 = 0xFFFFFFFF00000000ull;
    cf pe[2], a01[2], wg[2];
#pragma unroll
    for (int jp = 0; jp < 2; ++jp) {
      const u32x2 ex = __builtin_amdgcn_permlane16_swap(f2u(s[jp][15].x), f2u(s[jp][15].x), false, false);
      const u32x2 ey = __builtin_amdgcn_permlane16_swap(f2u(s[jp][15].y), f2u(s[jp][15].y), false, false);
      pe[jp] = cf{u2f(ex[0]), u2f(ey[0])};
      const cf ap = cmad(pe[jp], l16[jp], cf{u2f(ex[1]), u2f(ey[1])});
      const u32x2 hx = __builtin_amdgcn_permlane32_swap(f2u(ap.x), f2u(ap.x), false, false);
      const u32x2 hy = __builtin_amdgcn_permlane32_swap(f2u(ap.y), f2u(ap.y), false, false);
      a01[jp] = cf{u2f(hx[0]), u2f(hy[0])};
      wg[jp] = cmad(a01[jp], l32[jp], cf{u2f(hx[1]), u2f(hy[1])});
    }
    {
      const cf ws_ = sel_c(m1, wg[1], wg[0]);
      if (lane < 32)
        __builtin_amdgcn_raw_buffer_store_b128(u32x4{f2u(ws_.x), 1u, f2u(ws_.y), 1u},
                                               __builtin_amdgcn_make_buffer_rsrc((void*)(ws + WS_AGG), 0, 512 << 10, 0x00020000),
                                               ((2 * tm + wr) * 1024 + tn * 64 + wc * 32 + lane) * 16, 0, 16);
      if (wr == 0) {
        part[lane & 31] = ws_;
        wait_lgkm0();
      }
      lds_signal<SW_PUB>(U);
      if (wr) lds_wait<SW_PUB, 0>(U, 4);
    }
    __builtin_amdgcn_s_setprio(0);
    cf l2[2], l3[2], l4[2], c[2], pw[2];
#pragma unroll
    for (int jp = 0; jp < 2; ++jp) {
      const cf cof = prm[64 + jp * 16 + fr];
      l2[jp] = part[256 + jp * 16 + fr];
      l4[jp] = part[320 + jp * 16 + fr];
      const cf p1 = sel_c(m1, l16[jp], cf{1.f, 0.f});
      cf cc = sel_c(m1, pe[jp], cf{0.f, 0.f});
      cc = sel_c(m2, cmad(p1, a01[jp], cc), cc);
      cf p = sel_c(m2, cmul(p1, l32[jp]), p1);
      if (wr) {
        cc = cmad(p, part[jp * 16 + fr], cc);
        p = cmul(p, part[192 + jp * 16 + fr]);
      }
      pw[jp] = cmul(p, cof);
      c[jp] = cmul(cc, cof);
      l3[jp] = cmul(l2[jp], lam[jp]);
      asm volatile("" : "+v"(pw[jp]), "+v"(c[jp]), "+v"(l3[jp]), "+v"(l4[jp]));
#pragma unroll
      for (int i = 0; i < 16; ++i) {
        s[jp][i] = cmul(s[jp][i], cof);
        asm volatile("" : "+v"(s[jp][i]));
      }
    }
    __builtin_amdgcn_s_setprio(3);
    lds_wait<SW_G0E, 0>(U, 4);
    lds_wait<SW_GRP, 1>(U, 4);
    const __amdgpu_buffer_rsrc_t rs = __builtin_amdgcn_make_buffer_rsrc((void*)(ws + WS_HS), 0, 8 << 20, 0x00020000);
    const int hb = ((tm * 2 + wr) * 32 + tn * 2) * 8192 + fq * 2048 + (wc * 32 + fr) * 16;
    const uint32_t hl = lds_base(smem) + G0_STAGE + wr * 8192 + (fq ^ g1_rot(wr)) * 2048 + (wc * 32 + fr) * 16;
#pragma unroll
    for (int jp = 0; jp < 2; ++jp) {
      const int pl = wc * 32 + jp * 16 + fr;
      const cf hin = (grp[192 + pl] + grp[128 + pl]) + (grp[64 + pl] + grp[pl]);
      cf t = cmad(pw[jp], hin, c[jp]);
#pragma unroll
      for (int k = 0; k < 4; ++k) {
        s[jp][4 * k] = cmad(t, lam[jp], s[jp][4 * k]);
        s[jp][4 * k + 1] = cmad(t, l2[jp], s[jp][4 * k + 1]);
        s[jp][4 * k + 2] = cmad(t, l3[jp], s[jp][4 * k + 2]);
        t = cmul(t, l4[jp]);
        s[jp][4 * k + 3] = s[jp][4 * k + 3] + t;
      }
#pragma unroll
      for (int h = 0; h < 2; ++h) {
        u32x4 re, im;
#pragma unroll
        for (int i = 0; i < 4; ++i) {
          re[i] = pk_bf(s[jp][8 * h + 2 * i].x, s[jp][8 * h + 2 * i + 1].x);
          im[i] = pk_bf(-s[jp][8 * h + 2 * i].y, -s[jp][8 * h + 2 * i + 1].y);
        }
        __builtin_amdgcn_raw_buffer_store_b128(re, rs, (hb ^ (h * 64)) + jp * 256 + h * 1024, 0, 16);
        __builtin_amdgcn_raw_buffer_store_b128(im, rs, (hb ^ (h * 64)) + jp * 256 + h * 1024 + 8192, 0, 16);
        sta4((hl ^ (h * 64)) + jp * 256 + h * 1024, re);
        sta4((hl ^ (h * 64)) + jp * 256 + h * 1024 + G0_STAGE, im);
      }
    }
  }
  wait_lgkm0();
  lds_signal<SW_HS>(U);
  if (T.late) __builtin_amdgcn_s_setprio(0);
  publish(ws, 4 * tm + (T.late ? 2 : T.nt >> 2), lane);
  if (T.late) {
    bar();
    bar();
  }
}

__device__ __forceinline__ void gemm1(const Args& A, char* smem, int wave, int lane) {
  char* const ws = A.ws;
  const Tile T = tile_map();
  const int tm = T.tm, nt = T.nt;
  const uint32_t U = 4;
  const int wr = wave >> 1, wc = wave & 1, fr = lane & 15, fq = lane >> 4;
  f32x4 acc[4][4];
  if (T.late) {
    const char* px = reinterpret_cast<const char*>(A.xB + tm * 128 * 1024 + nt * 128 + wc * 64);
    const uint32_t pxo = (wr * 64 + g1_rot(wr) * 16 + fq * 4) * 2048 + fr * 8;
    const u32x2 dw = reinterpret_cast<const u32x2*>(A.Dsk + nt * 128 + wc * 64)[fr];
    u32x2 xw[16];
#pragma unroll
    for (int i = 0; i < 16; ++i) xw[i] = *reinterpret_cast<const u32x2*>(px + ((pxo ^ ((i >> 2) << 15)) + (i & 3) * 2048));
#pragma unroll
    for (int i = 0; i < 32; ++i) {
      const uint32_t a = xw[i >> 1][i & 1], d = dw[i & 1];
      const uint32_t p = pk_bf(u2f(a << 16) * u2f(d << 16), u2f(a & 0xFFFF0000u) * u2f(d & 0xFFFF0000u));
      acc[i >> 3][(i & 1) * 2][(i >> 1) & 3] = u2f(p << 16);
      acc[i >> 3][(i & 1) * 2 + 1][(i >> 1) & 3] = u2f(p & 0xFFFF0000u);
    }
#pragma unroll
    for (int m = 0; m < 4; ++m) asm volatile("" : "+v"(acc[m][0]), "+v"(acc[m][1]), "+v"(acc[m][2]), "+v"(acc[m][3]));
    lds_wait<SW_G0E, 2>(U, 4);
#pragma nounroll
    for (int k = 0; k < 3; ++k) {
      if (k == 2) lds_wait<SW_GRP, 2>(U, 4);
      stage_c(A, wave, lane, k);
    }
    wait_vm<0>();
    lds_signal<SW_CP>(U);
  } else {
#pragma unroll
    for (int i = 0; i < 16; ++i) acc[i >> 2][i & 3] = Z4;
  }
  {
    Core<true> g{acc};
    g.pa0 = lds_base(smem) + G0_STAGE + wr * 8192 + ((fr >> 1) & 1) * 1024 + (8 * fq + (fr >> 2) + (fr & 2) * 2) * 16 + (fr & 1) * 8;
    g.pa1 = g.pa0 ^ 64;
    g.pb0 = lds_base(smem) + G0_STAGE + 16384 + (wc * 64 + fr) * 128 + (fq ^ (fr & 7)) * 16;
    g.pb1 = g.pb0 ^ 64;
#pragma unroll
    for (int m = 0; m < 4; ++m) asm volatile("" : "+v"(acc[m][0]), "+v"(acc[m][1]), "+v"(acc[m][2]), "+v"(acc[m][3]));
    lds_wait<SW_CP, 0>(U, 4);
    lds_wait<SW_HS, 0>(U, 4);
    __builtin_amdgcn_s_setprio(1);
    g.run(0, T.late ? 32 - NX : NX, 32);
    const __amdgpu_buffer_rsrc_t rp = __builtin_amdgcn_make_buffer_rsrc((void*)(ws + WS_PS), 0, 8 << 20, 0x00020000);
    int po = bid_x() * 32768 + wave * 8192 + lane * 16;
    asm volatile("" : "+v"(po));
    auto part = [&](int m) {
      if (T.late) return;
      asm volatile("" : "+v"(acc[m][0]), "+v"(acc[m][1]), "+v"(acc[m][2]), "+v"(acc[m][3]));
#pragma unroll
      for (int i = 0; i < 2; ++i) {
        const f32x4 a = acc[m][2 * i], b = acc[m][2 * i + 1];
        __builtin_amdgcn_raw_buffer_store_b128(u32x4{pk_bf(a[0], b[0]), pk_bf(a[1], b[1]), pk_bf(a[2], b[2]), pk_bf(a[3], b[3])}, rp,
                                               po + (2 * m + i) * 1024, 0, 16);
      }
      __builtin_amdgcn_sched_barrier(0);
    };
#pragma unroll
    for (int i = 0; i < 32; ++i) {
      g.mm(i);
      if (i >= 12 && (i & 7) == 4) part((i >> 3) - 1);
    }
    asm volatile("s_nop 7" : "+v"(acc[3][0]), "+v"(acc[3][1]), "+v"(acc[3][2]), "+v"(acc[3][3]));
    part(3);
  }
  asm volatile("" : "+v"(lane));
  if (!T.late) {
    lds_signal<SW_PL>(U);
    publish(ws, 64 + bid_x(), lane);
    warm_dev(smem, ws + WS_PS, bid_x() * 32768 + wave * 8192 + lane * 128);
  } else {
    const __amdgpu_buffer_rsrc_t ro = __builtin_amdgcn_make_buffer_rsrc((void*)A.out, 0, 16 << 20, 0x00020000);
    const int o = ((tm * 128 + wr * 64 + g1_rot(wr) * 16 + (lane >> 4) * 4) * 2048 + 1024 + nt * 128 + wc * 64 + (lane & 15) * 4) * 4;
    const uint32_t pp = lds_base(smem) + wave * 8192 + lane * 16;
    lds_wait<SW_PL, 0>(U, 4);
    u32x4 p[8];
#pragma unroll
    for (int i = 0; i < 8; ++i) p[i] = ldsa(pp + i * 1024);
#pragma unroll
    for (int m = 0; m < 4; ++m) {
      u32x4 y[4];
#pragma unroll
      for (int i = 0; i < 16; ++i) {
        const int n = i & 3, r = i >> 2;
        const uint32_t w = p[2 * m + (n >> 1)][r];
        y[r][n] = f2u(acc[m][n][r] + u2f((n & 1) ? w & 0xFFFF0000u : w << 16));
      }
      asm volatile("" : "+v"(y[0]), "+v"(y[1]), "+v"(y[2]), "+v"(y[3]));
#pragma unroll
      for (int r = 0; r < 4; ++r) __builtin_amdgcn_raw_buffer_store_b128(y[r], ro, (o ^ (m << 17)) + r * 8192, 0, 16);
      __builtin_amdgcn_sched_barrier(0);
    }
  }
}

}

extern "C" __global__ __launch_bounds__(1024, 4) void k_all(const uint32_t* x2, const unsigned short* w_r, const unsigned short* w_i,
                                                           const unsigned short* xB, const unsigned short* Lre,
                                                           const unsigned short* Lim, const unsigned short* Bre,
                                                           const unsigned short* Bim, const unsigned short* Cre,
                                                           const unsigned short* Cim, const unsigned short* ldt,
                                                           const unsigned short* Dsk, float* out, char* ws) {
  __shared__ __attribute__((aligned(256))) char smem_[LDS_BYTES];
  char* const smem = smem_ + 256;
  const Args A{x2, w_r, w_i, xB, Lre, Lim, Bre, Bim, Cre, Cim, ldt, Dsk, out, ws};
  const int tid = tid_x(), lane = tid & 63;
  int wave = __builtin_amdgcn_readfirstlane(tid >> 6);
  const uint32_t ldsb = lds_base(smem);
  {
    const int o = lane * 128 + (lane >> 5) * ((bid_x() << 9) & 0x7000);
    asm volatile(
        "s_cmp_lg_u32 %1, %3\n\ts_cbranch_scc1 1f\n\ts_getpc_b64 s[90:91]\n\ts_add_u32 s90, s90, k_all@rel32@lo+4\n\t"
        "s_addc_u32 s91, s91, k_all@rel32@hi+12\n\ts_mov_b32 m0, %2\n\ts_nop 0\n\tglobal_load_lds_dword %0, s[90:91]\n1:"
        ::"v"(o < 33660 - 16 ? o : 33660 - 16), "s"(wave), "s"(ldsb + L_SCR2), "n"(0)
        : "s90", "s91", "scc", "memory");
  }
  if (wave < 8) {
    const uint32_t o = (2 * wave + 16 * (lane >> 3)) * 16384 + (bid_x() & 7) * 2048 + ((bid_x() >> 3) & 15) * 128 + ((lane & 7) ^ ((lane >> 5) * 4)) * 16;
    if (bid_x() >= 128)
      asm volatile(
          "s_mov_b32 m0, %4\n\ts_sleep " XSTR(0) "\n\tglobal_load_lds_dwordx4 %0, %2 nt\n\tglobal_load_lds_dwordx4 %1, %2 offset:1024 nt\n\t"
          "global_load_lds_dwordx4 %0, %3 offset:2048 nt\n\tglobal_load_lds_dwordx4 %1, %3 offset:3072 nt"
          ::"v"(o), "v"(o + (2 << 20) - 1024), "s"(x2), "s"(x2 + (16384 - 2048) / 4), "s"(ldsb + 3 * G0_STAGE + wave * 4096)
          : "memory");
    else
      stage0(A, smem, wave & 3, lane, 0, 1 + (wave >> 2));
  } else if (wave < 12) {
    const int wc = (bid_x() >> 3) & 15;
    if (bid_x() >= 128 && (wc & 7) != 3 && wave != 10)
      warm_r(A, smem, lane, wave == 11 ? (wc & 8) | 3 : wc, wave == 11 ? (wc & 7) - ((wc & 7) > 3) : wave - 8);
  } else if (wave == 15) {
    const uint32_t o = tile_map().tn * 128 + (lane & 31) * 4;
    asm volatile(
        "s_mov_b32 m0, %4\n\ts_mov_b32 exec_hi, 0\n\tglobal_load_lds_dword %0, %1\n\ts_add_u32 m0, m0, 128\n\ts_nop 0\n\t"
        "global_load_lds_dword %0, %2\n\ts_add_u32 m0, m0, 128\n\ts_nop 0\n\tglobal_load_lds_dword %0, %3\n\ts_mov_b32 exec_hi, -1"
        ::"v"(o), "s"(Lre), "s"(Lim), "s"(ldt), "s"(ldsb + L_PRM + 512)
        : "memory", "scc");
  } else if (wave == 13) {
    warm1(smem, Dsk, (lane & 15) * 128);
  }
  asm volatile("" : "+s"(wave));
  bool g1 = false;
  if (__builtin_expect(wave >= 12, 0)) {
    if (wave == 12) *(volatile lds_u32*)((lds_u32*)(smem + L_SYNC) + lane) = 0;
    mfma_role(A, smem, wave - 12, lane);
    g1 = bid_x() < 128;
  } else if (__builtin_expect(wave >= 8, 0)) {
    loader_role(A, smem, wave - 8, lane);
    __builtin_amdgcn_s_waitcnt(0x0F70);
  } else {
    g1 = fft_role(A, smem, wave, lane);
    __builtin_amdgcn_s_waitcnt(0x0F70);
  }
  if (g1) gemm1(A, smem, wave & 3, (int)__builtin_amdgcn_mbcnt_hi(~0u, __builtin_amdgcn_mbcnt_lo(~0u, 0u)));
}
#if defined(__HIP_DEVICE_COMPILE__) && __HIP_DEVICE_COMPILE__
#pragma clang attribute pop
#endif

extern "C" void kernel_launch(void* const* d_in, const int* in_sizes, int n_in, void* d_out, int out_size, void* d_ws,
                              size_t ws_size, hipStream_t stream) {
  typedef const unsigned short* P;
  k_all<<<256, 1024, 0, stream>>>((const uint32_t*)d_in[0], (P)d_in[1], (P)d_in[2], (P)d_in[3], (P)d_in[4], (P)d_in[5], (P)d_in[6],
                                  (P)d_in[7], (P)d_in[8], (P)d_in[9], (P)d_in[10], (P)d_in[11], (float*)d_out, (char*)d_ws);
}
```

```cpp
#include <hip/hip_runtime.h>
#include <stdint.h>

#define XSTR2(x) #x
#define XSTR(x) XSTR2(x)
namespace {

#if defined(__HIP_DEVICE_COMPILE__) && __HIP_DEVICE_COMPILE__
#pragma clang attribute push(__attribute__((target("no-packed-fp32-ops"))), apply_to = function)
#endif
__device__ __forceinline__ int tid_x() { return __builtin_amdgcn_workitem_id_x(); }
__device__ __forceinline__ int bid_x() { return __builtin_amdgcn_workgroup_id_x(); }

typedef float cf __attribute__((ext_vector_type(2)));
typedef short bf16x8 __attribute__((ext_vector_type(8)));
typedef short s16x4 __attribute__((ext_vector_type(4)));
typedef __bf16 b16x2 __attribute__((ext_vector_type(2)));
typedef float f32x4 __attribute__((ext_vector_type(4)));
typedef uint32_t u32x4 __attribute__((ext_vector_type(4)));
typedef uint32_t u32x2 __attribute__((ext_vector_type(2)));
typedef unsigned short u16;
typedef unsigned long long u64;
typedef __attribute__((address_space(3))) uint32_t lds_u32;

__device__ __forceinline__ float u2f(uint32_t u) { return __builtin_bit_cast(float, u); }
__device__ __forceinline__ uint32_t f2u(float f) { return __builtin_bit_cast(uint32_t, f); }
__device__ __forceinline__ float bf2f(u16 b) { return u2f((uint32_t)b << 16); }
__device__ __forceinline__ float rbf(float f) { return bf2f(__builtin_bit_cast(u16, (__bf16)f)); }
__device__ __forceinline__ uint32_t pk_bf(float a, float b) {
  const b16x2 h = b16x2{(__bf16)a, (__bf16)b};
  return __builtin_bit_cast(uint32_t, h);
}
__device__ __forceinline__ cf fin(cf v) {
  float x = v.x, y = v.y;
  asm("" : "+v"(x), "+v"(y));
  return cf{x, y};
}
__device__ __forceinline__ cf cmul(cf a, cf b) {
  return cf{__builtin_fmaf(a.x, b.x, -(a.y * b.y)), __builtin_fmaf(a.x, b.y, a.y * b.x)};
}
__device__ __forceinline__ cf cmad(cf a, cf b, cf c) {
  return cf{__builtin_fmaf(a.x, b.x, __builtin_fmaf(-a.y, b.y, c.x)), __builtin_fmaf(a.x, b.y, __builtin_fmaf(a.y, b.x, c.y))};
}
__device__ __forceinline__ cf cisr(int k, float inv_n) {
  const float r = (float)k * inv_n;
  return cf{__builtin_amdgcn_cosf(r), -__builtin_amdgcn_sinf(r)};
}
template <int N> __device__ __forceinline__ cf cis(int k) { return cisr(k, 1.0f / N); }
template <int N> __device__ __forceinline__ void wait_vm() { asm volatile("s_waitcnt vmcnt(%0)" ::"n"(N) : "memory"); }
__device__ __forceinline__ void wait_lgkm0() { asm volatile("s_waitcnt lgkmcnt(0)" ::: "memory"); }
__device__ __forceinline__ void glds2_dev(const void* sb, uint32_t v0, uint32_t lds_dst) {
  uint32_t keep;
  asm volatile(
      "s_mov_b32 %0, m0\n\ts_mov_b32 m0, %3\n\ts_nop 0\n\tglobal_load_lds_dwordx4 %1, %2 sc1\n\t"
      "global_load_lds_dwordx4 %1, %2 offset:1024 sc1\n\ts_mov_b32 m0, %0"
      : "=&s"(keep)
      : "v"(v0), "s"(sb), "s"(lds_dst)
      : "memory");
}
__device__ __forceinline__ void gdev2(const void* sb, uint32_t v0, uint32_t v1, uint32_t lds_dst) {
  uint32_t keep;
  asm volatile(
      "s_mov_b32 %0, m0\n\ts_mov_b32 m0, %4\n\ts_nop 0\n\tglobal_load_lds_dwordx4 %1, %3 sc1\n\t"
      "global_load_lds_dwordx4 %2, %3 offset:1024 sc1\n\ts_mov_b32 m0, %0"
      : "=&s"(keep)
      : "v"(v0), "v"(v1), "s"(sb), "s"(lds_dst)
      : "memory");
}
__device__ __forceinline__ void glds4(const void* sb0, const void* sb1, uint32_t v0, uint32_t v1, uint32_t v2, uint32_t v3,
                                      uint32_t lds_dst) {
  uint32_t keep;
  asm volatile(
      "s_mov_b32 %0, m0\n\ts_mov_b32 m0, %7\n\ts_nop 0\n\tglobal_load_lds_dwordx4 %1, %5\n\t"
      "global_load_lds_dwordx4 %2, %5 offset:1024\n\tglobal_load_lds_dwordx4 %3, %6 offset:2048\n\t"
      "global_load_lds_dwordx4 %4, %6 offset:3072\n\ts_mov_b32 m0, %0"
      : "=&s"(keep)
      : "v"(v0), "v"(v1), "v"(v2), "v"(v3), "s"(sb0), "s"(sb1), "s"(lds_dst)
      : "memory");
}
__device__ __forceinline__ constexpr uint32_t lds_base(char*) { return 256; }
__device__ __forceinline__ void bar() {
  __builtin_amdgcn_sched_barrier(0);
  asm volatile("" ::: "memory");
  __builtin_amdgcn_s_barrier();
  asm volatile("" ::: "memory");
  __builtin_amdgcn_sched_barrier(0);
}
__device__ __forceinline__ void bar_lds() {
  asm volatile("s_waitcnt lgkmcnt(0)" ::: "memory");
  bar();
}
template <int W> __device__ __forceinline__ void lds_signal(uint32_t U) {
  asm volatile("s_mov_b64 exec, 1\n\tds_add_u32 %0, %0 offset:%1\n\ts_mov_b64 exec, -1" ::"v"(U), "n"(W * 4 - 4) : "memory");
}
template <int W> __device__ __forceinline__ uint32_t lds_peek(uint32_t U) {
  return __builtin_amdgcn_readfirstlane(*(volatile lds_u32*)(size_t)(U + (W * 4 - 4)));
}
template <int W, int SLEEP> __device__ __forceinline__ void lds_wait(uint32_t U, uint32_t n) {
  uint32_t v, c;
  if (SLEEP)
    asm volatile(
        "s_branch 2f\n1:\n\ts_sleep %5\n2:\n\tds_read_b32 %0, %2 offset:%4\n\ts_waitcnt lgkmcnt(0)\n\tv_readfirstlane_b32 %1, %0\n\t"
        "s_cmp_lt_u32 %1, %3\n\ts_cbranch_scc1 1b"
        : "=&v"(v), "=&s"(c)
        : "v"(U), "s"(n * U), "n"(W * 4 - 4), "n"(SLEEP)
        : "memory", "scc");
  else
    asm volatile(
        "1:\n\tds_read_b32 %0, %2 offset:%4\n\ts_waitcnt lgkmcnt(0)\n\tv_readfirstlane_b32 %1, %0\n\t"
        "s_cmp_lt_u32 %1, %3\n\ts_cbranch_scc1 1b"
        : "=&v"(v), "=&s"(c)
        : "v"(U), "s"(n * U), "n"(W * 4 - 4)
        : "memory", "scc");
}
__device__ __forceinline__ float sel_f(uint64_t m, float a, float b) {
  float r;
  asm("v_cndmask_b32_e64 %0, %1, %2, %3" : "=v"(r) : "v"(b), "v"(a), "s"(m));
  return r;
}
__device__ __forceinline__ cf sel_c(uint64_t m, cf a, cf b) { return cf{sel_f(m, a.x, b.x), sel_f(m, a.y, b.y)}; }
__device__ __forceinline__ u64 ld_dev(const u64* p) { return __hip_atomic_load(p, __ATOMIC_RELAXED, __HIP_MEMORY_SCOPE_AGENT); }
__device__ __forceinline__ void st_dev(u64* p, u64 v) { __hip_atomic_store(p, v, __ATOMIC_RELAXED, __HIP_MEMORY_SCOPE_AGENT); }
__device__ __forceinline__ void st_dev(uint32_t* p, uint32_t v) {
  __hip_atomic_store(p, v, __ATOMIC_RELAXED, __HIP_MEMORY_SCOPE_AGENT);
}
__device__ __forceinline__ f32x4 mfma_b(u32x4 a, u32x4 b, f32x4 c) {
  return __builtin_amdgcn_mfma_f32_16x16x32_bf16(__builtin_bit_cast(bf16x8, a), __builtin_bit_cast(bf16x8, b), c, 0, 0, 0);
}
struct PQ {
  u32x4 p, q;
};
constexpr f32x4 Z4 = f32x4{0.f, 0.f, 0.f, 0.f};
constexpr float S16 = 0.283235f;
constexpr unsigned SPIN_MAX = 1u << 20;

constexpr size_t WS_EPOCH = 0;
constexpr size_t WS_F1 = 4096;
constexpr size_t WS_P2 = 8192;
constexpr size_t WS_HF = 1u << 19;
constexpr size_t WS_AGG = 1u << 20;
constexpr size_t WS_ZB = 2u << 20;
constexpr size_t WS_ZC = 8u << 20;
constexpr size_t WS_HS = 18u << 20;
constexpr size_t WS_PS = 26u << 20;

constexpr int NX = 14;
constexpr int G0_STAGE = 32768;
constexpr int L_Z = 0;
constexpr int L_X = 131072;
constexpr int L_TB = L_X + 16384;
constexpr int L_WS = L_TB + 4096;
constexpr int L_PART = L_WS + 4096;
constexpr int L_GRP = L_PART + 3584;
constexpr int L_RS = L_GRP + 2048;
constexpr int L_SYNC = -256;
constexpr int L_PRM = L_RS + 1024;
constexpr int L_SCR2 = L_PRM + 1024;
constexpr int LDS_BYTES = L_SCR2 + 256 + 256;
enum { SW_PART = 1, SW_GRP, SW_PUB, SW_CP, SW_S0, SW_HD, SW_G1, SW_FS, SW_G0E, SW_HS, SW_LD, SW_LD2, SW_F1, SW_PX, SW_FR, SW_PL, SW_P2 };

struct Args {
  const uint32_t* x2;
  const u16 *w_r, *w_i, *xB, *Lre, *Lim, *Bre, *Bim, *Cre, *Cim, *ldt, *Dsk;
  float* out;
  char* ws;
};

struct Tile {
  int tm, tn, nt, late;
};
__device__ __forceinline__ Tile tile_map() {
  const int bid = bid_x(), late = bid >> 7, cq = (bid & 1) * 4 + ((bid >> 5) & 3);
  return Tile{(bid >> 1) & 15, late * 8 + cq, cq, late};
}
__device__ __forceinline__ u32x4 lds4(const void* p) { return *reinterpret_cast<const u32x4*>(p); }
__device__ __forceinline__ u32x4 ldsa(uint32_t a) { return *(__attribute__((address_space(3))) u32x4*)(size_t)a; }
__device__ __forceinline__ void lds_cf2(const void* p, cf& a, cf& b) {
  const f32x4 v = *reinterpret_cast<const f32x4*>(p);
  a = cf{v[0], v[1]};
  b = cf{v[2], v[3]};
}
template <int W> __device__ __forceinline__ void group_sync(uint32_t U, uint32_t n) {
  wait_lgkm0();
  lds_signal<W>(U);
  lds_wait<W, 0>(U, n);
}
__device__ __forceinline__ int k_rot() { return (bid_x() & 6) * 2 + (bid_x() & 1) * 8; }
__device__ __forceinline__ void stage0(const Args& A, char* smem, int wave, int lane, int kt, int parts = 3) {
  const Tile T = tile_map();
  const int prow = lane >> 3;
  const uint32_t va = (T.tm * 128 + wave * 32 + prow) * 2048 + ((lane & 7) ^ (prow & 3)) * 16;
  const uint32_t vb = (T.tn * 64 + wave * 16 + prow) * 2048 + ((lane & 7) ^ prow) * 16;
  const uint32_t dst = lds_base(smem) + wave * 4096 + (kt & 3) * G0_STAGE;
  kt = (kt + k_rot()) & 15;
  if (parts & 1) glds4(A.xB + kt * 64, A.xB + kt * 64, va, va + 15360, (va ^ 64) + 2 * 15360, (va ^ 64) + 3 * 15360, dst);
  if (parts & 2) glds4(A.Bre + kt * 64, A.Bim + kt * 64 - 1024, vb, vb + 15360, vb, vb + 15360, dst + 16384);
}
__device__ __forceinline__ int g1_kt(int late, int s) {
  const int c = 2 * tile_map().nt, g = c & 8;
  if (late && s >= 2 && s < 10) return (g ^ 8) + s - 2;
  if (s < 2) return late * 16 + c + s;
  if (s < 8) return g + (s - 2 < (c & 7) ? s - 2 : s);
  const int i = late ? s - 10 : s + 14 - NX;
  return 16 + (i < c ? i : i + 2);
}
__device__ __forceinline__ int g1_rot(int wr) { return 1 ? (tile_map().nt ^ (wr << 1)) & 3 : 0; }
__device__ __forceinline__ uint32_t g1_vc(int wave, int lane) {
  return (tile_map().nt * 128 + (wave >> 1) * 64 + (wave & 1) * 2 + (lane >> 3) * 4) * 2048 + ((lane & 7) ^ (lane >> 3)) * 16;
}
__device__ __forceinline__ void stage_c(const Args& A, int wave, int lane, int s) {
  const int kt = g1_kt(tile_map().late, s);
  const u16* src = ((kt & 1) ? A.Cim : A.Cre) + (kt >> 1) * 64;
  const uint32_t vc = g1_vc(wave, lane);
  glds4(src, src, vc, vc + 65536 - 1024, vc, vc + 65536 - 1024, 256 + (s + 1) * G0_STAGE + 16384 + wave * 4096);
}
__device__ __forceinline__ void warm1(char* smem, const void* base, uint32_t off) {
  uint32_t keep;
  asm volatile("s_mov_b32 %0, m0\n\ts_mov_b32 m0, %3\n\ts_nop 0\n\tglobal_load_lds_dword %1, %2\n\ts_mov_b32 m0, %0"
               : "=&s"(keep)
               : "v"(off), "s"(base), "s"(lds_base(smem) + L_SCR2)
               : "memory");
}
__device__ __forceinline__ void warm_dev(char* smem, const void* base, uint32_t off) {
  uint32_t keep;
  asm volatile("s_mov_b32 %0, m0\n\ts_mov_b32 m0, %3\n\ts_nop 0\n\tglobal_load_lds_dword %1, %2 sc1\n\ts_mov_b32 m0, %0"
               : "=&s"(keep)
               : "v"(off), "s"(base), "s"(lds_base(smem) + L_SCR2)
               : "memory");
}
__device__ __forceinline__ void warm_r(const Args& A, char* smem, int lane, int c, int i) {
  const int bid = bid_x(), tm = ((bid >> 1) & 3) + (c & 3) * 4, nt = (bid & 1) * 4 + (c >> 2);
  uint64_t px = (uint64_t)A.xB, pbr = (uint64_t)A.Bre, pbi = (uint64_t)A.Bim;
  asm volatile("" : "+s"(px), "+s"(pbr), "+s"(pbi));
  px += (tm * 128 + (nt & 3) * 32 + (lane & 31)) * 2048;
  const uint64_t pb = ((lane & 16) ? pbi : pbr) + (nt * 64 + (c & 3) * 16 + (lane & 15)) * 2048;
  const int hi = lane >> 5;
  const bool k2 = i == 4 || i == 6;
  const int sl = i < 3 ? i + 1 : (k2 ? i - 4 : i + 1) + hi;
  const uint64_t pfp = ((k2 || (i < 3 && hi)) ? pb + (k2 ? 1 << 20 : 0) : px) + ((sl + k_rot()) & 15) * 128;
  uint32_t keep;
  asm volatile("s_mov_b32 %0, m0\n\ts_mov_b32 m0, %2\n\ts_nop 0\n\tglobal_load_lds_dword %1, off\n\ts_mov_b32 m0, %0"
               : "=&s"(keep)
               : "v"(pfp), "s"(lds_base(smem) + L_SCR2)
               : "memory");
}
__device__ __forceinline__ u32x2 lds_tr(uint32_t a) {
  return __builtin_bit_cast(u32x2, __builtin_amdgcn_ds_read_tr16_b64_v4i16((__attribute__((address_space(3))) s16x4*)(size_t)a));
}
__device__ __forceinline__ void sta4(uint32_t a, u32x4 v) { *(__attribute__((address_space(3))) u32x4*)(size_t)a = v; }
template <int P> __device__ __forceinline__ void prio(bool on) {
  __builtin_amdgcn_sched_barrier(0);
  if (on) __builtin_amdgcn_s_setprio(P);
  __builtin_amdgcn_sched_barrier(0);
}
__device__ __forceinline__ void row_head(u32x4 (&x)[2][2], const PQ& m16, const PQ& m1, const cf (&tw)[2][4], bool on) {
  f32x4 dr[2][2], di[2][2];
#pragma unroll
  for (int k = 0; k < 2; ++k) {
    const f32x4 ur = mfma_b(x[k][0], m16.p, Z4), ui = mfma_b(x[k][0], m16.q, Z4);
    const u32x4 xn = x[k][1] ^ 0x80008000u;
    dr[k][0] = mfma_b(x[k][1], m1.p, ur), di[k][0] = mfma_b(x[k][1], m1.q, ui);
    dr[k][1] = mfma_b(xn, m1.p, ur), di[k][1] = mfma_b(xn, m1.q, ui);
  }
  prio<0>(on);
#pragma unroll
  for (int i = 0; i < 16; ++i) {
    const int k = i >> 3, t = (i >> 2) & 1, r = i & 3;
    const cf v = fin(cmul(cf{dr[k][t][r], di[k][t][r]}, tw[t][r]));
    dr[k][t][r] = v.x, di[k][t][r] = v.y;
  }
  prio<1>(on);
#pragma unroll
  for (int i = 0; i < 16; ++i) x[i >> 3][(i >> 2) & 1][i & 3] = pk_bf(dr[i >> 3][(i >> 2) & 1][i & 3], di[i >> 3][(i >> 2) & 1][i & 3]);
}
template <int W, bool B = false, bool D = false>
__device__ __forceinline__ void poll_count(char* ws, int idx, uint32_t n, uint32_t U, const void* src = nullptr, uint32_t v0 = 0, uint32_t v1 = 0,
                                           uint32_t dst = 0) {
  uint32_t c0, c1, c2, t, lim = SPIN_MAX;
#define ASK(c) "global_atomic_add " c ", %5, %5, %6 sc0\n\t"
#define CHK(c) "s_waitcnt vmcnt(2)\n\tv_readfirstlane_b32 %3, " c "\n\ts_cmp_ge_u32 %3, %7\n\ts_cbranch_scc1 2f\n\t" ASK(c)
  asm volatile(
      "s_mov_b64 exec, 1\n\t" ASK("%0") "s_sleep 4\n\t" ASK("%1") "s_sleep 4\n\t" ASK("%2") ".if %10\n\ts_barrier\n.endif\n1:\n\t" CHK("%0") CHK("%1") CHK("%2")
      "s_sub_u32 %4, %4, 1\n\ts_cbranch_scc0 1b\n2:\n\tds_add_u32 %8, %8 offset:%9\n\ts_mov_b64 exec, -1\n.if %11\n\t"
      "s_mov_b32 %3, m0\n\ts_mov_b32 m0, %15\n\ts_nop 0\n\tglobal_load_lds_dwordx4 %13, %12 sc1\n\tglobal_load_lds_dwordx4 %13, %12 offset:1024 sc1\n\t"
      "global_load_lds_dwordx4 %14, %12 offset:2048 sc1\n\tglobal_load_lds_dwordx4 %14, %12 offset:3072 sc1\n\ts_mov_b32 m0, %3\n\t"
      "s_waitcnt vmcnt(4)\n.else\n\ts_waitcnt vmcnt(0)\n.endif"
      : "=&v"(c0), "=&v"(c1), "=&v"(c2), "=&s"(t), "+&s"(lim)
      : "v"(0), "s"(ws + WS_HF + idx * 128), "s"(n), "v"(U), "n"(W * 4 - 4), "n"(B), "n"(D), "s"(D ? src : ws), "v"(v0), "v"(v1), "s"(dst)
      : "memory", "scc");
#undef ASK
#undef CHK
}
__device__ __forceinline__ bool fft_role(const Args& A, char* smem, int f, int lane) {
  char* const ws = A.ws;
  const Tile T = tile_map();
  const int bid = bid_x(), li = bid & 127;
  int l15 = lane & 15, q = lane >> 4;
  lds_u32* const sw = (lds_u32*)(smem + L_SYNC);
  const uint32_t U = (uint32_t)(size_t)(sw + 1), ldsb = lds_base(smem);
  const __amdgpu_buffer_rsrc_t rzb = __builtin_amdgcn_make_buffer_rsrc((void*)(ws + WS_ZB), 0, 16 << 20, 0x00020000);
  auto fsync = [&](int n) { group_sync<SW_FS>(U, 8 * n); };
  auto bars = [&](int n) {
#pragma nounroll
    for (int i = 0; i < n; ++i) bar();
  };
  u64 epoch = 0;
  if (f == 0) epoch = ld_dev((const u64*)(ws + WS_EPOCH));
  if (f == 7) {
    const __amdgpu_buffer_rsrc_t ra = __builtin_amdgcn_make_buffer_rsrc((void*)(ws + WS_AGG), 0, 512 << 10, 0x00020000);
    __builtin_amdgcn_raw_buffer_store_b128(u32x4{0u, 0u, 0u, 0u}, ra, (T.tm * 2048 + T.tn * 64 + lane) * 16, 0, 16);
    __builtin_amdgcn_raw_buffer_store_b128(u32x4{0u, 0u, 0u, 0u}, ra, (T.tm * 2048 + 1024 + T.tn * 64 + lane) * 16, 0, 16);
    if (!T.late && lane < (T.tn ? 1 : 4)) st_dev((uint32_t*)(ws + WS_HF + (lane ? 4 * T.tm + lane - 1 : 64 + li) * 128), 0u);
    if (lane == 0) st_dev((uint32_t*)(ws + WS_P2) + bid, 0u);
  }
  __builtin_amdgcn_sched_barrier(0);
  auto ks_of = [&](int w) { return bid ? bid + 256 * w : (w < 4 ? 256 * w : 256 * w - 896); };
  {
    const int e = f & 3, n = 4 * q + e;
    uint32_t* tb = reinterpret_cast<uint32_t*>(smem + L_TB) + lane * 4;
    uint32_t* tx = reinterpret_cast<uint32_t*>(smem + L_X) + lane * 4;
    if (f < 4) {
      const cf c = cis<16>(n * l15) * S16;
      tb[e] = pk_bf(c.x, -c.y);
      tb[256 + e] = pk_bf(c.y, c.x);
      if (!T.late) {
        const cf d = cis<32>((2 * n + 1) * l15) * S16;
        tx[e] = pk_bf(d.x, -d.y);
        tx[256 + e] = pk_bf(d.y, d.x);
      }
    }
    if (!T.late) {
      *reinterpret_cast<cf*>(tx + 512 + (f >> 1) * 256 + (f & 1) * 2) = cis<512>(n * (16 * (f >> 2) + l15)) * (1.0f / (64 * S16 * S16));
      const cf w = cis<1024>(l15 + 16 * (f >> 2) + 32 * n);
      *reinterpret_cast<cf*>(tx + 1536 + (f >> 1) * 256 + (f & 1) * 2) = cf{w.y, -w.x};
      const int slot = f < 4 ? l15 : n, u = f < 4 ? q >> 1 : l15 & 1, j = f < 4 ? n & 7 : l15 >> 1;
      const bool own = (bid | (slot & 7)) == 0;
      const cf g = cis<2048>(j * (own ? slot * 128 : ks_of(slot & 7)));
      const int ua = bid ? 0 : (slot >> 2) & 1, ub = bid ? 1 : ua;
      const float al = u == ua ? ((own && f < 4) ? 1.f : 0.5f) : 0.f, be = (u == ub && !own) ? (slot < 8 ? 0.5f : -0.5f) : 0.f;
      const float sa = al + be, da = al - be;
      tx[2560 + (f >> 2) * 512 + e] = pk_bf(sa * g.x, -(f < 4 ? da : sa) * g.y);
      tx[2816 + (f >> 2) * 512 + e] = pk_bf((f < 4 ? sa : da) * g.y, da * g.x);
    }
  }
  if (!T.late) {
    if (f == 0) wait_vm<1>(); else if (f == 7) wait_vm<4>(); else wait_vm<0>();
  }
  bar_lds();
  PQ m16, m1, m8;
  cf tw[2][4];
  u32x4 wv[4];
  m16.p = lds4(smem + L_TB + lane * 16), m16.q = lds4(smem + L_TB + 1024 + lane * 16);
  PQ mk[2];
  auto col_mats = [&](bool b) {
#pragma unroll
    for (int i = 0; i < 8; ++i) {
      const cf c = cis<256>((4 * q + (i & 3)) * (16 * l15 + 2 * f + (i >> 2))) * (1.0f / (16 * S16));
      uint32_t p = pk_bf(c.x, -c.y), o = pk_bf(c.y, c.x);
      asm volatile("" : "+v"(p), "+v"(o));
      mk[i >> 2].p[i & 3] = p, mk[i >> 2].q[i & 3] = o;
      if (b && (i & 1)) bar();
    }
    asm volatile("" : "+v"(mk[0].p), "+v"(mk[0].q), "+v"(mk[1].p), "+v"(mk[1].q));
  };
  auto col256 = [&](uint32_t img, int n, bool cj) {
    typedef __attribute__((address_space(3))) u32x2 lds_u32x2;
    const uint32_t own = img + f * 4096 + q * 512, xi = own + (q & 1) * 64 + l15 * 4;
    u32x4 x[4], d[4];
#pragma unroll
    for (int i = 0; i < 16; ++i) x[i >> 2][i & 3] = *(lds_u32*)(size_t)((xi ^ ((i & 4) * 16)) + (i >> 3) * 2048 + (i & 3) * 128);
    wait_lgkm0();
    lds_signal<SW_FS>(U);
#pragma unroll
    for (int i = 0; i < 4; ++i) {
      const f32x4 re = mfma_b(m16.p, x[i], Z4), im = mfma_b(m16.q, x[i], Z4);
      d[i] = u32x4{pk_bf(re[0], im[0]), pk_bf(re[1], im[1]), pk_bf(re[2], im[2]), pk_bf(re[3], im[3])};
    }
    const uint32_t xo = img + q * 8192 + (f >> 1) * 512 + l15 * 16 + (f & 1) * 8;
    asm volatile("" : "+v"(d[0]), "+v"(d[1]), "+v"(d[2]), "+v"(d[3]));
    lds_wait<SW_FS, 0>(U, 8 * n);
#pragma unroll
    for (int i = 0; i < 8; ++i) *(lds_u32x2*)(size_t)(xo + (i & 3) * 2048 + (i >> 2) * 256) = u32x2{d[i >> 2][i & 3], d[2 + (i >> 2)][i & 3]};
    fsync(n + 1);
#pragma unroll
    for (int i = 0; i < 4; ++i) x[i] = ldsa(own + l15 * 16 + (i >> 1) * 2048 + (i & 1) * 256);
    const uint32_t xt = img + f * 4096 + l15 * 128 + (q ^ (l15 & 7)) * 16;
#pragma unroll
    for (int i = 0; i < 4; ++i) {
      const f32x4 re = mfma_b(x[i], mk[i >> 1].p, Z4);
      f32x4 im = mfma_b(x[i], mk[i >> 1].q, Z4);
      if (cj) im = -im;
      d[i] = u32x4{pk_bf(re[0], im[0]), pk_bf(re[1], im[1]), pk_bf(re[2], im[2]), pk_bf(re[3], im[3])};
    }
    asm volatile("" : "+v"(d[0]), "+v"(d[1]), "+v"(d[2]), "+v"(d[3])::"memory");
#pragma unroll
    for (int i = 0; i < 4; ++i) sta4((xt ^ ((i & 1) * 64)) + (i >> 1) * 2048, d[i]);
    asm volatile("" ::: "memory");
  };
  if (T.late) {
    col_mats(false);
    if (f == 0) wait_vm<1>(); else if (f == 7) wait_vm<3>(); else wait_vm<0>();
    col256(ldsb + 3 * G0_STAGE, 1, false);
    const uint32_t xl = ldsb + 3 * G0_STAGE + f * 4096 + (lane >> 3) * 128 + ((lane ^ (lane >> 3)) & 7) * 16;
#pragma unroll
    for (int i = 0; i < 4; ++i) wv[i] = ldsa(xl + i * 1024);
    const int zo = (2 * f + 16 * (lane >> 3)) * 16384 + (li & 7) * 2048 + (li >> 3) * 128 + (lane & 7) * 16;
    asm volatile("" : "+v"(wv[0]), "+v"(wv[1]), "+v"(wv[2]), "+v"(wv[3]));
#pragma unroll
    for (int i = 0; i < 4; ++i) __builtin_amdgcn_raw_buffer_store_b128(wv[i], rzb, zo + (i >> 1) * 16384 + (i & 1) * (2 << 20), 0, 16);
    bars(2);
  } else {
    const char* tab = smem + L_X + lane * 16;
    m1.p = lds4(tab), m1.q = lds4(tab + 1024), m8.p = lds4(tab + 10240), m8.q = lds4(tab + 11264);
#pragma unroll
    for (int i = 0; i < 4; ++i) lds_cf2(tab + 2048 + i * 1024, tw[i >> 1][(i & 1) * 2], tw[i >> 1][(i & 1) * 2 + 1]);
  }
  wait_vm<0>();
  asm volatile("" : "+v"(epoch));
  const u64 tag = (epoch + 1) | (1ull << 63);
  u64 f1 = tag;
  asm volatile("" : "+v"(f1));
  bar();
  if (f == 0 && lane == 0) st_dev((u64*)(ws + WS_F1) + bid, f1);
  int nb = T.late ? 10 : 11;
  if (T.late && f >= 6) {
    uint64_t pbr = (uint64_t)A.Bre, pbi = (uint64_t)A.Bim;
    asm volatile("" : "+s"(pbr), "+s"(pbi));
    uint64_t pf = ((lane & 16) ? pbi : pbr) + (T.tn * 64 + (T.tm >> 2) * 16 + l15) * 2048 + ((2 * f + (lane >> 5) + k_rot()) & 15) * 128;
    asm volatile("" : "+v"(pf));
    bars(f - 4);
    nb -= f - 4;
    __builtin_amdgcn_s_sleep(5);
    uint32_t keep;
    asm volatile("s_mov_b32 %0, m0\n\ts_mov_b32 m0, %2\n\ts_nop 0\n\tglobal_load_lds_dword %1, off\n\ts_mov_b32 m0, %0"
                 : "=&s"(keep)
                 : "v"(pf), "s"(ldsb + L_SCR2)
                 : "memory");
  }
  cf plam, pcof, lp[11];
  if (f == 2) {
    const u16* raw = reinterpret_cast<const u16*>(smem + L_PRM + 512);
    uint32_t pr = raw[lane], pi = raw[64 + lane], pd = raw[128 + lane];
    asm volatile("" : "+v"(pr), "+v"(pi), "+v"(pd)::"memory");
    auto expf_ = [](float x) {
      const float t = x * 1.44269504088896341f;
      const float r = __builtin_fmaf(x, 1.44269504088896341f, -t) + x * 1.92596299112661746e-8f;
      const float e = __builtin_amdgcn_exp2f(t);
      return __builtin_fmaf(e * r, 0.693147180559945309f, e);
    };
    const float lr = u2f(pr << 16), li = u2f(pi << 16);
    float dt = rbf(expf_(u2f(pd << 16)));
    asm volatile("" : "+v"(dt));
    bar();
    const float zr = dt * lr, zi = dt * li;
    const float e = expf_(zr);
    const float rh = zi * 0.159154936671257019f;
    const float rf = (rh - __builtin_rintf(rh)) + __builtin_fmaf(zi, 6.42063832653e-9f, __builtin_fmaf(zi, 0.159154936671257019f, -rh));
    plam = cf{e * __builtin_amdgcn_cosf(rf), e * __builtin_amdgcn_sinf(rf)};
    asm volatile("" : "+v"(plam));
    bar();
    float den = 2.0f / (lr * lr + li * li);
    asm volatile("" : "+v"(den));
    bar();
    const float nr = plam.x - 1.0f, ni = plam.y;
    pcof = cf{(nr * lr + ni * li) * den, (ni * lr - nr * li) * den};
    lp[0] = cmul(plam, plam);
#pragma unroll
    for (int k = 1; k < 4; ++k) lp[k] = cmul(lp[k - 1], lp[k - 1]);
    asm volatile("" : "+v"(pcof), "+v"(lp[0]), "+v"(lp[1]), "+v"(lp[3]));
    bar();
#pragma unroll
    for (int k = 4; k < 10; ++k) lp[k] = cmul(lp[k - 1], lp[k - 1]);
    lp[10] = cmul(lp[8], lp[9]);
    asm volatile("" : "+v"(lp[4]), "+v"(lp[5]), "+v"(lp[6]), "+v"(lp[8]), "+v"(lp[9]), "+v"(lp[10]));
    nb -= 4;
  }
  bars(nb);
  u64 v[4];
  auto poll1 = [&]() {
#pragma unroll
    for (int i = 0; i < 4; ++i) v[i] = ld_dev((const u64*)(ws + WS_F1) + lane + 64 * i);
  };
  if (f == 0) {
    __builtin_amdgcn_s_sleep(5);
    poll1();
  }
  int zo = (q < 2 ? bid : (bid ? 256 - bid : 128)) * 16384 + (q & 1) * 8192 + f * 256 + l15 * 16;
  asm volatile("" : "+v"(zo));
  bar();
  if (T.late && f >= 4) {
    __builtin_amdgcn_s_sleep(6);
    warm1(smem, (f & 1) ? A.Cre : A.Cim, (T.nt * 32 + T.tm * 2 + ((f >> 1) & 1)) * 8192 + lane * 128);
  }
  if (f == 2) {
    cf* prm = reinterpret_cast<cf*>(smem + L_PRM);
    prm[lane] = plam;
    prm[64 + lane] = pcof;
    cf* pt = reinterpret_cast<cf*>(smem + L_PART) + lane;
    pt[64] = lp[3], pt[128] = lp[4], pt[192] = lp[5], pt[256] = lp[0], pt[320] = lp[1], pt[384] = lp[6];
    pt = reinterpret_cast<cf*>(smem + L_GRP) + lane;
    pt[0] = cf{1.f, 0.f}, pt[64] = lp[8], pt[128] = lp[9], pt[192] = lp[10];
    wait_lgkm0();
    lds_signal<SW_PART>(U);
    lds_signal<SW_LD2>(U);
  }
  if (f == 0) {
    __builtin_amdgcn_s_setprio(2);
#pragma nounroll
    for (unsigned spins = 0; spins < SPIN_MAX; ++spins) {
      bool ok = true;
#pragma unroll
      for (int i = 0; i < 4; ++i) ok &= v[i] == tag;
      if (__builtin_amdgcn_ballot_w64(!ok) == 0) break;
      __builtin_amdgcn_s_sleep(16);
      poll1();
    }
    lds_signal<SW_F1>(U);
    asm volatile("" ::"v"(v[0]), "v"(v[1]), "v"(v[2]), "v"(v[3]));
  }
  u32x4 zi[4];
  if (!T.late) {
    if (f) lds_wait<SW_F1, 1>(U, 1);
#pragma unroll
    for (int e = 0; e < 4; ++e) zi[e] = __builtin_amdgcn_raw_buffer_load_b128(rzb, zo + e * 2048, 0, 16);
  }
  if (f == 0) __builtin_amdgcn_s_setprio(0);
  if (f == 0 && lane == 0) {
    if (T.late) st_dev((uint32_t*)(ws + WS_P2) + bid, 256u);
    if (bid == 0) st_dev((u64*)(ws + WS_EPOCH), epoch + 1);
  }
  if ((bid | f) == 0) __builtin_amdgcn_s_setprio(3);
  if (!T.late) {
    const __amdgpu_buffer_rsrc_t rr = __builtin_amdgcn_make_buffer_rsrc((void*)A.w_r, 0, 2048 * 1026, 0x00020000);
    const __amdgpu_buffer_rsrc_t ri = __builtin_amdgcn_make_buffer_rsrc((void*)A.w_i, 0, 2048 * 1026, 0x00020000);
    uint32_t sr, si;
    auto wload = [&](int ka) {
      const int oa = ka * 1026, ob = ((2048 - ka) & 2047) * 1026;
      wv[0] = __builtin_amdgcn_raw_buffer_load_b128(rr, lane * 16, oa & ~3, 2);
      wv[1] = __builtin_amdgcn_raw_buffer_load_b128(ri, lane * 16, oa & ~3, 2);
      wv[2] = __builtin_amdgcn_raw_buffer_load_b128(rr, lane * 16, (ob + 2) & ~3, 2);
      wv[3] = __builtin_amdgcn_raw_buffer_load_b128(ri, lane * 16, (ob + 2) & ~3, 2);
      const int so = lane == 0 ? ob : (lane == 1 ? oa + 1024 : ((ka & 1) ? oa + 1022 : ob + 1024));
      sr = __builtin_amdgcn_raw_buffer_load_b16(rr, so, 0, 2);
      si = __builtin_amdgcn_raw_buffer_load_b16(ri, so, 0, 2);
    };
    const bool two = (bid | f) == 0;
    u32x4 w2r, w2i;
    uint32_t s2r, s2i;
    if (two) {
      wload(1024);
      w2r = wv[0], w2i = wv[1], s2r = sr, s2i = si;
    }
    wload(ks_of(f));
    __builtin_amdgcn_sched_barrier(0);
    {
      u32x4 o[4];
      asm volatile("" : "+v"(zi[0]), "+v"(zi[1]), "+v"(zi[2]), "+v"(zi[3]));
#pragma unroll
      for (int c = 0; c < 4; ++c) {
        const u32x4 a = u32x4{zi[0][c], zi[1][c], zi[2][c], zi[3][c]};
        const f32x4 re = mfma_b(a, m8.p, Z4), im = mfma_b(a, m8.q, Z4);
#pragma unroll
        for (int r = 0; r < 4; ++r) o[r][c] = pk_bf(re[r], im[r]);
      }
      const uint32_t oa = l15 * 2048 + f * 256 + ((4 * q) ^ (l15 & 7) ^ ((f & 2) << 1)) * 16;
#pragma unroll
      for (int r = 0; r < 4; ++r) sta4(ldsb + L_Z + (oa ^ (r * 16)), o[r]);
      wait_lgkm0();
      lds_signal<SW_FS>(U);
    }
    auto shift = [](u32x4& w, uint32_t last) {
      const uint32_t nx = (uint32_t)__builtin_amdgcn_update_dpp((int)last, (int)(uint32_t)w[0], 0x130, 0xF, 0xF, false);
      w = u32x4{__builtin_amdgcn_alignbit(w[1], w[0], 16), __builtin_amdgcn_alignbit(w[2], w[1], 16),
                __builtin_amdgcn_alignbit(w[3], w[2], 16), __builtin_amdgcn_alignbit(nx, w[3], 16)};
    };
    uint32_t wsp = 0;
    if (two) {
      sta4(ldsb + L_WS + lane * 16, w2r), sta4(ldsb + L_WS + 1024 + lane * 16, w2i);
      shift(w2r, s2r), shift(w2i, s2i);
      sta4(ldsb + L_WS + 2048 + lane * 16, w2r), sta4(ldsb + L_WS + 3072 + lane * 16, w2i);
      wsp = s2r | s2i << 16;
    }
    asm volatile("" : "+v"(wsp));
    lds_wait<SW_FS, 0>(U, 8);
    prio<1>(!two);
    const uint32_t row = ldsb + L_Z + f * 2048;
    u32x4 x[2][2];
    auto rows = [&]() {
      const uint32_t ra = row + q * 512 + ((l15 >> 2) ^ f ^ ((q & 1) << 2)) * 16 + (l15 & 3) * 4;
#pragma unroll
      for (int i = 0; i < 16; ++i) x[i >> 3][(i >> 2) & 1][i & 3] = *(lds_u32*)(size_t)((ra ^ ((i & 4) * 16)) + (i & 3) * 128 + (i >> 3) * 16384);
    };
    rows();
    if (two) x[1][0] = x[0][0] & 0xFFFF0000u, x[1][1] = x[0][1] & 0xFFFF0000u, x[0][0] &= 0xFFFFu, x[0][1] &= 0xFFFFu;
    uint32_t wa = row, wb = two ? ldsb + L_X + 14336 : row + 16384;
    asm volatile("" : "+v"(x[0][0]), "+v"(x[0][1]), "+v"(x[1][0]), "+v"(x[1][1])::"memory");
    row_head(x, m16, m1, tw, !two);
    if (bid & 1) shift(wv[0], sr), shift(wv[1], si);
    else shift(wv[2], sr), shift(wv[3], si);
    wsp = (uint32_t)__builtin_amdgcn_update_dpp((int)(sr | si << 16), (int)wsp, 0x114, 0xF, 0x2, false);
    sta4(wa + lane * 16, wv[0]);
    sta4(wa + 1024 + lane * 16, wv[1]);
    sta4(wb + lane * 16, wv[2]);
    sta4(wb + 1024 + lane * 16, wv[3]);
    asm volatile("" : "+v"(wsp)::"memory");
#pragma nounroll
    for (int jb = 0;; jb = 4) {
      u32x2 fw[4][2];
#pragma unroll
      for (int h = 0; h < 2; ++h) {
        fw[0][h] = lds_tr(wa + q * 256 + l15 * 8 + h * 128);
        fw[1][h] = lds_tr(wa + q * 256 + l15 * 8 + h * 128 + 1024);
        fw[2][h] = lds_tr(wb + (3 - q) * 256 + l15 * 8 + h * 128);
        fw[3][h] = lds_tr(wb + (3 - q) * 256 + l15 * 8 + h * 128 + 1024);
      }
#pragma unroll
      for (int i = 0; i < 8; ++i)
        fw[2 + (i >> 2)][(i >> 1) & 1][i & 1] =
            (uint32_t)__builtin_amdgcn_update_dpp(0, (int)(uint32_t)fw[2 + (i >> 2)][(i >> 1) & 1][i & 1], 0x140, 0xF, 0xF, true);
      const uint32_t s0 = __builtin_amdgcn_readlane(wsp, jb), s1 = __builtin_amdgcn_readlane(wsp, jb + 1);
#pragma unroll
      for (int t = 0; t < 2; ++t) {
        cf u[4];
        lds_cf2(smem + L_X + 6144 + t * 2048 + lane * 16, u[0], u[1]), lds_cf2(smem + L_X + 7168 + t * 2048 + lane * 16, u[2], u[3]);
        f32x4 ar = mfma_b(m16.p, x[0][t], Z4), ai = mfma_b(m16.q, x[0][t], Z4), br = mfma_b(m16.p, x[1][t], Z4), bi = mfma_b(m16.q, x[1][t], Z4);
        auto half = [](uint32_t w, int hi) { return u2f(hi ? w & 0xFFFF0000u : w << 16); };
        cf lo[4];
#pragma unroll
        for (int r = 0; r < 4; ++r) {
          const int rr = t ? 3 - r : r;
          lo[r] = fin(cf{half(fw[2 * t][rr >> 1][rr & 1], 0), half(fw[2 * t + 1][rr >> 1][rr & 1], 0)});
        }
        prio<0>(!two);
#pragma unroll
        for (int r = 0; r < 4; ++r) {
          const int rr = t ? r : 3 - r;
          const cf hi = cf{half(fw[2 - 2 * t][rr >> 1][rr & 1], 1), half(fw[3 - 2 * t][rr >> 1][rr & 1], 1)};
          cf fa = t ? hi : lo[r], fb = t ? lo[r] : hi;
          fb.y = -fb.y;
          if (t == 0 && r == 0 && lane == 0) {
            fa = (fa + cf{half(s0, 0), -half(s0, 1)}) * 0.5f;
            fb = (fb + cf{half(s1, 0), half(s1, 1)}) * 0.5f;
          }
          const cf X1 = cmad(u[r], cf{br[r], bi[r]}, cf{ar[r], ai[r]});
          const cf X2 = cf{__builtin_fmaf(2.f, ar[r], -X1.x), __builtin_fmaf(2.f, ai[r], -X1.y)};
          const cf Yb = cmul(X2, fb), S = fin(cmad(X1, fa, Yb));
          const cf V = fin(cmul(cf{u[r].x, -u[r].y}, cf{__builtin_fmaf(-2.f, Yb.x, S.x), __builtin_fmaf(-2.f, Yb.y, S.y)}));
          ar[r] = S.x, ai[r] = S.y, br[r] = V.x, bi[r] = V.y;
        }
        prio<1>(!two);
        if (two) ar += br, ai += bi;
#pragma unroll
        for (int r = 0; r < 4; ++r) x[0][t][r] = pk_bf(ar[r], -ai[r]), x[1][t][r] = pk_bf(br[r], -bi[r]);
      }
      if (!two) break;
      if (jb) {
        x[1][0] = x[0][0], x[1][1] = x[0][1];
        x[0][0] = ldsa(row + lane * 16), x[0][1] = ldsa(row + 1024 + lane * 16);
        break;
      }
      sta4(row + lane * 16, x[0][0]), sta4(row + 1024 + lane * 16, x[0][1]);
      rows();
      x[0][0] = x[1][0] & 0xFFFFu, x[0][1] = x[1][1] & 0xFFFFu, x[1][0] &= 0xFFFF0000u, x[1][1] &= 0xFFFF0000u;
      row_head(x, m16, m1, tw, false);
      wa = ldsb + L_WS, wb = ldsb + L_WS + 2048;
    }
    row_head(x, m16, m1, tw, !two);
    const uint32_t oa = row + l15 * 128 + (q ^ (l15 & 7)) * 16;
#pragma unroll
    for (int i = 0; i < 4; ++i) {
      const f32x4 re = mfma_b(x[i >> 1][i & 1], m16.p, Z4), im = mfma_b(x[i >> 1][i & 1], m16.q, Z4);
      sta4((oa ^ ((i & 1) * 64)) + (i >> 1) * 16384, u32x4{pk_bf(re[0], im[0]), pk_bf(re[1], im[1]), pk_bf(re[2], im[2]), pk_bf(re[3], im[3])});
    }
    m8.p = lds4(smem + L_X + 12288 + lane * 16), m8.q = lds4(smem + L_X + 13312 + lane * 16);
    fsync(2);
    u32x4 yi[4];
#pragma unroll
    for (int e = 0; e < 4; ++e) yi[e] = lds4(smem + L_Z + (4 * q + e) * 2048 + f * 256 + (l15 ^ ((2 * f + (l15 >> 3)) & 7)) * 16);
    asm volatile("" : "+v"(yi[0]), "+v"(yi[1]), "+v"(yi[2]), "+v"(yi[3]));
    u32x4 o[4];
#pragma unroll
    for (int c = 0; c < 4; ++c) {
      const u32x4 y = u32x4{yi[0][c], yi[1][c], yi[2][c], yi[3][c]};
      const f32x4 re = mfma_b(m8.p, y, Z4), im = mfma_b(m8.q, y, Z4);
#pragma unroll
      for (int r = 0; r < 4; ++r) o[r][c] = pk_bf(re[r], im[r]);
    }
    const int zc = (int)(WS_ZC - WS_ZB) + q * 4096 + f * 256 + l15 * 16;
    asm volatile("" : "+v"(o[0]), "+v"(o[1]), "+v"(o[2]), "+v"(o[3]));
    __builtin_amdgcn_s_setprio(0);
#pragma unroll
    for (int i = 0; i < 4; ++i)
      __builtin_amdgcn_raw_buffer_store_b128(o[i], rzb, zc + ((i & 1) ? (bid ? 256 - bid : 128) : bid) * 16384 + (i >> 1) * 2048, 0, 16);
  } else {
    if (f >= 4) return true;
    if (f == 1) {
      lds_wait<SW_PUB, 4>(U, 4);
      lds_wait<SW_F1, 4>(U, 1);
      __builtin_amdgcn_s_sleep(10);
      poll_count<SW_G1>(ws, 4 * T.tm + 1 - (T.nt >> 2), 16, U);
    }
  }
  const int N = T.late ? 32 - NX : NX;
  if (T.late && f != 1) return false;
  bars(2);
  wait_vm<0>();
  bar();
  if (f == 1) {
    if (T.late) bars(2);
    {
      uint32_t c;
      asm volatile("s_mov_b64 exec, 1\n\tglobal_atomic_add %0, %1, %1, %2 sc0\n\ts_mov_b64 exec, -1\n\ts_barrier\n\ts_waitcnt vmcnt(0)"
                   : "=&v"(c)
                   : "v"(0), "s"(ws + WS_HF + (4 * T.tm + 2) * 128)
                   : "memory");
      if (__builtin_amdgcn_readfirstlane(c) < 32u) poll_count<SW_FR>(ws, 4 * T.tm + 2, 32, U);
    }
    if (T.late) {
      bars(N - 11);
      poll_count<SW_PX, true>(ws, 64 + li, 4, U);
      return false;
    }
    bars(NX - 8);
  } else if (f == 0) {
    if (lane == 0) st_dev((uint32_t*)(ws + WS_P2) + bid, 256u);
    bars(NX - 7 - 2);
    const __amdgpu_buffer_rsrc_t rp = __builtin_amdgcn_make_buffer_rsrc((void*)(ws + WS_P2), 0, 1024, 0x00020000);
    u32x4 pw = __builtin_amdgcn_raw_buffer_load_b128(rp, lane * 16, 0, 16);
    bars(2);
#pragma nounroll
    for (unsigned spins = 0; spins < SPIN_MAX; ++spins) {
      if (__builtin_amdgcn_ballot_w64((pw[0] & pw[1] & pw[2] & pw[3] & 256u) == 0) == 0) break;
      __builtin_amdgcn_s_sleep(8);
      pw = __builtin_amdgcn_raw_buffer_load_b128(rp, lane * 16, 0, 16);
      asm volatile("" ::: "memory");
    }
    lds_signal<SW_P2>(U);
    asm volatile("" ::"v"(pw));
    if (lane < 2) st_dev((u64*)(ws + WS_F1) + bid + lane * 128, 0ull);
  } else {
    bars(NX - 7);
  }
  {
    asm volatile("" : "+v"(lane));
    l15 = lane & 15;
    q = lane >> 4;
    constexpr uint32_t img = 256;
    const uint32_t vo = (2 * f + 16 * (lane >> 3)) * 16384 + (li & 7) * 2048 + (li >> 3) * 128 + ((lane & 7) ^ ((lane >> 5) * 4)) * 16;
    if (f) lds_wait<SW_P2, 1>(U, 1);
    gdev2(ws + WS_ZC, vo, vo + (2 << 20) - 1024, img + f * 4096);
    gdev2(ws + WS_ZC + 16384, vo, vo + (2 << 20) - 1024, img + f * 4096 + 2048);
    col_mats(false);
    wait_vm<0>();
    col256(img, 3, true);
    typedef __attribute__((address_space(3))) u32x2 lds_u32x2;
    const uint32_t xr = img + f * 4096 + q * 128 + ((l15 >> 1) ^ q) * 16 + (l15 & 1) * 8;
    u32x2 z[8];
#pragma unroll
    for (int i = 0; i < 8; ++i) z[i] = *(lds_u32x2*)(size_t)((xr ^ ((i & 1) * 64)) + i * 512);
    const __amdgpu_buffer_rsrc_t ro = __builtin_amdgcn_make_buffer_rsrc((void*)A.out, 0, 16 << 20, 0x00020000);
    const int oo = (2 * f + 16 * q) * 65536 + (li & 7) * 8192 + (li >> 3) * 256 + l15 * 16;
    lds_wait<SW_PL, 2>(U, 4);
#pragma unroll
    for (int i = 0; i < 8; ++i) {
      const uint32_t p0 = z[i][0], p1 = z[i][1];
      __builtin_amdgcn_raw_buffer_store_b128(u32x4{p0 << 16, p0 & 0xFFFF0000u, p1 << 16, p1 & 0xFFFF0000u}, ro, oo + (i >> 2) * 65536 + (i & 3) * (4 << 20), 0, 16);
    }
  }
  return false;
}

__device__ __forceinline__ void publish(char* ws, int idx, int lane) {
  wait_vm<0>();
  if (lane == 0) __hip_atomic_fetch_add((uint32_t*)(ws + WS_HF + idx * 128), 1u, __ATOMIC_RELAXED, __HIP_MEMORY_SCOPE_AGENT);
}
__device__ __forceinline__ void loader_role(const Args& A, char* smem, int wave, int lane) {
  char* const ws = A.ws;
  const Tile T = tile_map();
  const int tm = T.tm, tn = T.tn;
  lds_u32* const sw = (lds_u32*)(smem + L_SYNC);
  const uint32_t U = (uint32_t)(size_t)(sw + 1);
  const uint32_t ldsb = lds_base(smem), lds0 = ldsb + wave * 4096;
  const int prow = lane >> 3;
  {
    __builtin_amdgcn_s_setprio(0);
    const uint32_t va = (tm * 128 + wave * 32 + prow) * 2048 + ((lane & 7) ^ (prow & 3)) * 16;
    const uint32_t vb = (tn * 64 + wave * 16 + prow) * 2048 + ((lane & 7) ^ prow) * 16;
    if (T.late) {
      bar();
      const int wc = (bid_x() >> 3) & 15;
      if ((wc & 7) != 3) {
#pragma nounroll
        for (int i = 2 + wave; i < 7; i += 4) warm_r(A, smem, lane, wc, i);
      }
      stage0(A, smem, wave, lane, 0);
    }
    stage0(A, smem, wave, lane, 1);
    {
      uint32_t k = 2, o = ((2 + k_rot()) & 15) * 128, so = 2 * G0_STAGE;
      const u64 bx = (u64)A.xB, br = (u64)A.Bre, bi = (u64)A.Bim - 2048;
      asm volatile(
          "s_add_u32 s88, %[xl], %[o]\n\ts_addc_u32 s89, %[xh], 0\n\ts_add_u32 m0, %[l0], %[so]\n1:\n\ts_waitcnt vmcnt(8)\n\ts_barrier\n\t"
          "global_load_lds_dwordx4 %[a0], s[88:89]\n\tglobal_load_lds_dwordx4 %[a1], s[88:89] offset:1024\n\t"
          "global_load_lds_dwordx4 %[a2], s[88:89] offset:2048\n\tglobal_load_lds_dwordx4 %[a3], s[88:89] offset:3072\n\t"
          "s_add_u32 m0, m0, 16384\n\ts_add_u32 s84, %[brl], %[o]\n\ts_addc_u32 s85, %[brh], 0\n\t"
          "s_add_u32 s86, %[bil], %[o]\n\ts_addc_u32 s87, %[bih], 0\n\ts_add_u32 %[o], %[o], 128\n\ts_and_b32 %[o], %[o], 0x780\n\t"
          "s_add_u32 s88, %[xl], %[o]\n\ts_addc_u32 s89, %[xh], 0\n\t"
          "s_add_u32 %[so], %[so], 0x8000\n\ts_and_b32 %[so], %[so], 0x18000\n\ts_add_u32 %[k], %[k], 1\n\t"
          "global_load_lds_dwordx4 %[b0], s[84:85]\n\tglobal_load_lds_dwordx4 %[b1], s[84:85] offset:1024\n\t"
          "global_load_lds_dwordx4 %[b0], s[86:87] offset:2048\n\tglobal_load_lds_dwordx4 %[b1], s[86:87] offset:3072\n\t"
          "s_add_u32 m0, %[l0], %[so]\n\ts_cmp_lt_u32 %[k], 16\n\ts_cbranch_scc1 1b"
          : [k] "+&s"(k), [o] "+&s"(o), [so] "+&s"(so)
          : [a0] "v"(va), [a1] "v"(va + 15360), [a2] "v"((va ^ 64) + 2 * 15360), [a3] "v"((va ^ 64) + 3 * 15360), [b0] "v"(vb), [b1] "v"(vb + 15360),
            [xl] "s"((uint32_t)bx), [xh] "s"((uint32_t)(bx >> 32)), [brl] "s"((uint32_t)br), [brh] "s"((uint32_t)(br >> 32)),
            [bil] "s"((uint32_t)bi), [bih] "s"((uint32_t)(bi >> 32)), [l0] "s"(lds0)
          : "memory", "scc", "s84", "s85", "s86", "s87", "s88", "s89"
      );
    }
    wait_vm<8>();
    lds_signal<SW_LD>(U);
    wait_vm<0>();
    lds_signal<SW_LD2>(U);
  }
  __builtin_amdgcn_s_setprio(2);
  {
    const int nseg = 2 * tm - 8 * wave;
    cf G = cf{0.f, 0.f};
    lds_wait<SW_PART, 2>(U, 1);
    lds_wait<SW_F1, 2>(U, 1);
    if (nseg > 0) {
      const __amdgpu_buffer_rsrc_t ra = __builtin_amdgcn_make_buffer_rsrc((void*)(ws + WS_AGG), 0, 512 << 10, 0x00020000);
      int o[8];
#pragma unroll
      for (int k = 0; k < 8; ++k) {
        o[k] = ((nseg - 1 - k) * 1024 + tn * 64 + lane) * 16;
        asm volatile("" : "+v"(o[k]));
      }
      u32x4 u[8];
      cf l64 = reinterpret_cast<const cf*>(smem + L_PART)[192 + lane], l128 = reinterpret_cast<const cf*>(smem + L_PART)[384 + lane];
      cf lw = reinterpret_cast<const cf*>(smem + L_GRP)[wave * 64 + lane];
      asm volatile("" : "+v"(l64), "+v"(l128), "+v"(lw));
      lds_wait<SW_PUB, 1>(U, 4);
      __builtin_amdgcn_s_sleep(10);
#pragma nounroll
      for (unsigned spins = 0; spins < SPIN_MAX; ++spins) {
        uint32_t n = 0;
#pragma unroll
        for (int k = 0; k < 8; ++k) u[k] = __builtin_amdgcn_raw_buffer_load_b128(ra, o[k], 0, 16);
#pragma unroll
        for (int k = 0; k < 8; ++k) n += u[k][1] + u[k][3];
        if (__builtin_amdgcn_ballot_w64(n != 2u * (nseg < 8 ? nseg : 8)) == 0) break;
        asm volatile("" ::: "memory");
      }
#pragma unroll
      for (int k = 6; k >= 0; k -= 2)
        G = cmad(G, l128, cmad(cf{u2f(u[k + 1][0]), u2f(u[k + 1][2])}, l64, cf{u2f(u[k][0]), u2f(u[k][2])}));
      G = cmul(G, lw);
    }
    reinterpret_cast<cf*>(smem + L_GRP)[wave * 64 + lane] = G;
    wait_lgkm0();
    lds_signal<SW_GRP>(U);
  }
  int tv = g1_kt(T.late, lane & 31) & 31;
  asm volatile("" : "+v"(tv));
  const char* ab = ws + WS_HS - 2048 + (size_t)tm * (64 * 8192);
  const uint32_t vh2 = ((wave >> 1) * 262144 + (wave & 1) * 4096 + 2048 + lane * 16) ^ (g1_rot(wave >> 1) * 2048), vh = (vh2 ^ 2048) + 2048;
  if (!T.late) {
    lds_wait<SW_GRP, 2>(U, 4);
    lds_wait<SW_G0E, 2>(U, 4);
    stage_c(A, wave, lane, 0);
    if (wave == 0) {
      stage_c(A, wave, lane, 1);
      wait_vm<0>();
      lds_signal<SW_CP>(U);
    }
    lds_wait<SW_HS, 2>(U, 4);
    if (wave == 0) {
      __builtin_amdgcn_s_sleep(12);
      poll_count<SW_G1, false, true>(ws, 4 * tm + (T.nt >> 2), 16, U, ab + g1_kt(0, 2) * 8192, vh, vh2, lds0 + 3 * G0_STAGE);
    } else {
      stage_c(A, wave, lane, 1);
    }
    stage_c(A, wave, lane, 2);
    if (wave) {
      wait_vm<4>();
      lds_signal<SW_CP>(U);
    }
  }
  lds_wait<SW_G1, 1>(U, 1);
  __builtin_amdgcn_s_setprio(0);
  const int N = T.late ? 32 - NX : NX;
  const uint32_t vc = g1_vc(wave, lane);
  if (T.late || wave) {
    const char* pa = ab + g1_kt(T.late, 2) * 8192;
    glds2_dev(pa, vh, lds0 + 3 * G0_STAGE);
    glds2_dev(pa + 2048, vh2, lds0 + 3 * G0_STAGE + 2048);
  }
  lds_wait<SW_S0, 1>(U, 4);
  {
    uint32_t k = 3, kt = g1_kt(T.late, 3), dst = lds0 + G0_STAGE, t;
    asm volatile(
        "s_lshl_b32 %[t], %[kt], 13\n\ts_add_u32 s86, %[al], %[t]\n\ts_addc_u32 s87, %[ah], 0\n\t"
        "s_mov_b32 m0, %[dst]\n\ts_branch 2f\n1:\n\ts_waitcnt vmcnt(8)\n\ts_barrier\n2:\n\t"
        "global_load_lds_dwordx4 %[h0], s[86:87] sc1\n\tglobal_load_lds_dwordx4 %[h0], s[86:87] offset:1024 sc1\n\t"
        "global_load_lds_dwordx4 %[h1], s[86:87] offset:2048 sc1\n\tglobal_load_lds_dwordx4 %[h1], s[86:87] offset:3072 sc1\n\t"
        "s_add_u32 m0, m0, 16384\n\ts_bitcmp0_b32 %[kt], 0\n\ts_cselect_b64 s[84:85], %[cr], %[ci]\n\ts_lshl_b32 %[t], %[kt], 6\n\t"
        "s_and_b32 %[t], %[t], 0x780\n\ts_add_u32 s84, s84, %[t]\n\ts_addc_u32 s85, s85, 0\n\ts_add_u32 %[k], %[k], 1\n\t"
        "v_readlane_b32 %[kt], %[tv], %[k]\n\ts_add_u32 %[t], %[dst], 0x8000\n\ts_cmp_eq_u32 %[dst], %[dl]\n\ts_cselect_b32 %[dst], %[df], %[t]\n\t"
        "s_lshl_b32 %[t], %[kt], 13\n\ts_add_u32 s86, %[al], %[t]\n\ts_addc_u32 s87, %[ah], 0\n\t"
        "global_load_lds_dwordx4 %[c0], s[84:85]\n\tglobal_load_lds_dwordx4 %[c1], s[84:85] offset:1024\n\t"
        "global_load_lds_dwordx4 %[c0], s[84:85] offset:2048\n\tglobal_load_lds_dwordx4 %[c1], s[84:85] offset:3072\n\t"
        "s_mov_b32 m0, %[dst]\n\ts_cmp_lt_u32 %[k], %[n]\n\ts_cbranch_scc1 1b"
        : [k] "+&s"(k), [kt] "+&s"(kt), [dst] "+&s"(dst), [t] "=&s"(t)
        : [h0] "v"(vh), [h1] "v"(vh2), [c0] "v"(vc), [c1] "v"(vc + 65536 - 1024), [tv] "v"(tv), [cr] "s"(A.Cre), [ci] "s"(A.Cim),
          [al] "s"((uint32_t)(u64)ab), [ah] "s"((uint32_t)((u64)ab >> 32)), [df] "s"(lds0 + G0_STAGE), [dl] "s"(lds0 + 3 * G0_STAGE), [n] "s"(N)
        : "memory", "scc", "s84", "s85", "s86", "s87"
    );
  }
  wait_vm<8>();
  lds_signal<SW_LD>(U);
  wait_vm<0>();
  lds_signal<SW_LD2>(U);
  if (T.late) {
    const char* ps = ws + WS_PS + (size_t)(bid_x() & 127) * 32768 + wave * 8192;
    lds_wait<SW_PX, 1>(U, 1);
#pragma nounroll
    for (int i = 0; i < 4; ++i) glds2_dev(ps + i * 2048, lane * 16, ldsb + wave * 8192 + i * 2048);
    wait_vm<0>();
    lds_signal<SW_PL>(U);
  }
}

template <bool TR> struct Core {
  struct AF {
    u32x2 l, h;
  };
  f32x4 (&acc)[4][4];
  uint32_t pa0, pa1, pb0, pb1;
  AF a[2][2];
  int so = 0;
  u32x4 b[4][2];
  static constexpr int DA = TR ? 2048 : 512;
  static constexpr int pn(int i) { return (i >> 3) == 1 ? 3 - ((i >> 1) & 3) : (i >> 1) & 3; }
  static constexpr int pk(int i) { return (i ^ (i >> 1)) & 1; }
  __device__ __forceinline__ void ra(int m, int j) {
    AF& d = a[m & 1][j >> 1];
    if (TR) {
      if (j & 1) d.h = lds_tr(pa1 + m * DA + (j >> 1) * 512); else d.l = lds_tr(pa0 + m * DA + (j >> 1) * 512);
    } else if ((j & 1) == 0) {
      const u32x4 v = ldsa((j ? pa1 : pa0) + m * DA);
      d.l = u32x2{v[0], v[1]}, d.h = u32x2{v[2], v[3]};
    }
    __builtin_amdgcn_sched_barrier(0);
  }
  __device__ __forceinline__ void rb(int i) {
    b[pn(i)][pk(i)] = ldsa((pk(i) ? pb1 : pb0) + pn(i) * 2048);
    __builtin_amdgcn_sched_barrier(0);
  }
  __device__ __forceinline__ void mm(int i) {
    const AF& x = a[(i >> 3) & 1][pk(i)];
    const u32x4 av = u32x4{x.l[0], x.l[1], x.h[0], x.h[1]};
    asm volatile("v_mfma_f32_16x16x32_bf16 %0, %1, %2, %0" : "+v"(acc[i >> 3][pn(i)]) : "v"(av), "v"(b[pn(i)][pk(i)]));
    __builtin_amdgcn_sched_barrier(0);
    if (i < 4) ra(1, i);
    if (i >= 8 && i < 8 + 4) ra(2, i - 8);
    if (i >= 15 && i < 19) ra(3, i - 15);
  }
  static __device__ __forceinline__ void add(uint32_t& p, int adv) {
    p += adv;
    asm volatile("" : "+v"(p));
    __builtin_amdgcn_sched_barrier(0);
  }
  template <int BAR> __device__ __forceinline__ void step(int& k, int k1, uint32_t cnt) {
    int adv;
#pragma unroll
    for (int i = 0; i < 32; ++i) {
      mm(i);
      if (i == 12)
        asm volatile("s_cmp_eq_u32 %1, %2\n\ts_cselect_b32 %0, %3, %4" : "=s"(adv) : "s"(so), "n"((TR ? 2 : 3) * G0_STAGE), "s"((TR ? -2 : -3) * G0_STAGE), "s"(G0_STAGE) : "scc");
      if (i == 13) asm volatile("s_add_u32 %0, %0, %2\n\ts_add_u32 %1, %1, 1" : "+s"(so), "+s"(k) : "s"(adv) : "scc");
      if (i == 14) add(pb0, adv);
      if (i == 19) add(pa0, adv);
      if (i == 20) add(pa1, adv);
      if (i == 21) add(pb1, adv);
      if (i == 23) __builtin_amdgcn_s_waitcnt(0xC07F);
      if (i == 23 && BAR == 1) asm volatile("s_barrier" ::: "memory");
      if (i == 23 && BAR == 2) {
        lds_signal<SW_S0>(4);
        __builtin_amdgcn_s_setprio(1);
        __builtin_amdgcn_sched_barrier(0);
      }
      if (i == 23 && !BAR) {
        uint32_t c, t;
        asm volatile(
            "s_sub_u32 %3, %4, %5\n\ts_lshl_b32 %3, %3, 2\n\tv_mov_b32 %1, %3\n\ts_add_u32 %3, %3, %6\n1:\n\tds_read_b32 %0, %1 offset:%7\n\t"
            "s_waitcnt lgkmcnt(0)\n\tv_readfirstlane_b32 %2, %0\n\ts_cmp_lt_u32 %2, %3\n\ts_cbranch_scc1 1b"
            : "=&v"(a[0][0].l[0]), "=&v"(a[0][0].l[1]), "=&s"(c), "=&s"(t)
            : "s"(k), "s"(k1), "s"(cnt), "n"(SW_LD * 4)
            : "scc", "memory");
        __builtin_amdgcn_sched_barrier(0);
      }
      __builtin_amdgcn_sched_barrier(0);
      if (i == 24) rb(i);
      if (i >= 24 && i < (TR ? 28 : 26)) ra(0, TR ? i - 24 : 2 * (i - 24));
      if (i > 24) rb(i);
    }
  }
  __device__ __forceinline__ void run(int k, int n, uint32_t cnt) {
    const int k1 = k + n - 3;
    asm volatile("" : "+v"(pa0), "+v"(pa1), "+v"(pb0), "+v"(pb1), "+s"(so));
    if (!TR) bar_lds();
    ra(0, 0), ra(0, 1), rb(0), ra(0, 2), ra(0, 3);
#pragma unroll
    for (int i = 1; i < 8; ++i) rb(i);
    if (TR) {
#pragma nounroll
      do step<2>(k, 0, 0); while (k == 0);
    }
#pragma nounroll
    do step<1>(k, 0, 0); while (k != k1);
#pragma nounroll
    do step<0>(k, k1 + 1, cnt); while (k != k1 + 2);
  }
};

__device__ __forceinline__ void mfma_role(const Args& A, char* smem, int wave, int lane) {
  char* const ws = A.ws;
  const Tile T = tile_map();
  const int tm = T.tm, tn = T.tn;
  lds_u32* const sw = (lds_u32*)(smem + L_SYNC);
  const uint32_t U = (uint32_t)(size_t)(sw + 1);
  const int wr = wave >> 1, wc = wave & 1;
  int fr = lane & 15, fq = lane >> 4;
  {
    if (T.late) bar_lds();
    if (wave == 3) wait_vm<0>();
    __builtin_amdgcn_s_setprio(1);
  }
  {
    f32x4 acc[4][4];
#pragma unroll
    for (int m = 0; m < 4; ++m)
#pragma unroll
      for (int n = 0; n < 4; ++n) acc[m][n] = Z4;
    const cf* prm = reinterpret_cast<const cf*>(smem + L_PRM) + wc * 32;
    cf* part = reinterpret_cast<cf*>(smem + L_PART) + wc * 32;
    const cf* grp = reinterpret_cast<const cf*>(smem + L_GRP);
    cf lam[2], l16[2], l32[2], s[2][16];
    auto scan = [&](int i, int jp) {
      const int m = i >> 2;
      if ((i & 3) == 0 || i == 1) asm volatile("" : "+v"(acc[m][2 * jp]), "+v"(acc[m][2 * jp + 1]));
      if (i == 1) s[jp][0] = cf{acc[0][2 * jp][0], acc[0][2 * jp + 1][0]};
      float x = acc[m][2 * jp][i & 3], y = acc[m][2 * jp + 1][i & 3];
      asm volatile("v_fma_f32 %0, -%3, %5, %0\n\tv_fmac_f32 %1, %3, %4\n\tv_fmac_f32 %0, %2, %4\n\tv_fmac_f32 %1, %2, %5"
                   : "+v"(x), "+v"(y)
                   : "v"(s[jp][i - 1].x), "v"(s[jp][i - 1].y), "v"(lam[jp].x), "v"(lam[jp].y));
      s[jp][i] = cf{x, y};
      __builtin_amdgcn_sched_barrier(0);
    };
    {
      Core<false> g{acc};
      g.pa0 = lds_base(smem) + (wr * 64 + (fr >> 2) * 16 + (fr & 3)) * 128 + (fq ^ (fr & 7)) * 16;
      g.pb0 = lds_base(smem) + (128 + wc * 64 + fr) * 128 + (fq ^ (fr & 7)) * 16;
      g.pa1 = g.pa0 ^ 64, g.pb1 = g.pb0 ^ 64;
#pragma unroll
      for (int m = 0; m < 4; ++m) asm volatile("" : "+v"(acc[m][0]), "+v"(acc[m][1]), "+v"(acc[m][2]), "+v"(acc[m][3]));
      g.run(0, 16, 16);
#pragma unroll
      for (int i = 0; i < 32; ++i) {
        g.mm(i);
        if (i == 1) {
          asm volatile("" : "+v"(lane));
          lam[0] = prm[lane & 15], lam[1] = prm[16 + (lane & 15)];
          __builtin_amdgcn_s_setprio(3);
          __builtin_amdgcn_sched_barrier(0);
        }
        if (i >= 8 && i < 14) scan(1 + (i - 8) % 3, (i - 8) / 3);
        if (i >= 14 && i < 22) scan(4 + ((i - 14) & 3), i < 18);
        if (i >= 22) scan(8 + ((i - 22) & 3) + (i >= 30 ? 4 : 0), i >= 26 && i < 30);
        if (i == 25) {
          lds_signal<SW_G0E>(U);
          asm volatile("" : "+v"(lane));
          fr = lane & 15;
          fq = lane >> 4;
#pragma unroll
          for (int jp = 0; jp < 2; ++jp) l16[jp] = part[64 + jp * 16 + fr], l32[jp] = part[128 + jp * 16 + fr];
          __builtin_amdgcn_sched_barrier(0);
        }
      }
      scan(14, 0), scan(15, 0);
#pragma unroll
      for (int i = 12; i < 16; ++i) scan(i, 1);
    }
    const uint64_t m1 = 0xFFFF0000FFFF0000ull, m2 = 0xFFFFFFFF00000000ull;
    cf pe[2], a01[2], wg[2];
#pragma unroll
    for (int jp = 0; jp < 2; ++jp) {
      const u32x2 ex = __builtin_amdgcn_permlane16_swap(f2u(s[jp][15].x), f2u(s[jp][15].x), false, false);
      const u32x2 ey = __builtin_amdgcn_permlane16_swap(f2u(s[jp][15].y), f2u(s[jp][15].y), false, false);
      pe[jp] = cf{u2f(ex[0]), u2f(ey[0])};
      const cf ap = cmad(pe[jp], l16[jp], cf{u2f(ex[1]), u2f(ey[1])});
      const u32x2 hx = __builtin_amdgcn_permlane32_swap(f2u(ap.x), f2u(ap.x), false, false);
      const u32x2 hy = __builtin_amdgcn_permlane32_swap(f2u(ap.y), f2u(ap.y), false, false);
      a01[jp] = cf{u2f(hx[0]), u2f(hy[0])};
      wg[jp] = cmad(a01[jp], l32[jp], cf{u2f(hx[1]), u2f(hy[1])});
    }
    {
      const cf ws_ = sel_c(m1, wg[1], wg[0]);
      if (lane < 32)
        __builtin_amdgcn_raw_buffer_store_b128(u32x4{f2u(ws_.x), 1u, f2u(ws_.y), 1u},
                                               __builtin_amdgcn_make_buffer_rsrc((void*)(ws + WS_AGG), 0, 512 << 10, 0x00020000),
                                               ((2 * tm + wr) * 1024 + tn * 64 + wc * 32 + lane) * 16, 0, 16);
      if (wr == 0) {
        part[lane & 31] = ws_;
        wait_lgkm0();
      }
      lds_signal<SW_PUB>(U);
      if (wr) lds_wait<SW_PUB, 0>(U, 4);
    }
    __builtin_amdgcn_s_setprio(0);
    cf l2[2], l3[2], l4[2], c[2], pw[2];
#pragma unroll
    for (int jp = 0; jp < 2; ++jp) {
      const cf cof = prm[64 + jp * 16 + fr];
      l2[jp] = part[256 + jp * 16 + fr];
      l4[jp] = part[320 + jp * 16 + fr];
      const cf p1 = sel_c(m1, l16[jp], cf{1.f, 0.f});
      cf cc = sel_c(m1, pe[jp], cf{0.f, 0.f});
      cc = sel_c(m2, cmad(p1, a01[jp], cc), cc);
      cf p = sel_c(m2, cmul(p1, l32[jp]), p1);
      if (wr) {
        cc = cmad(p, part[jp * 16 + fr], cc);
        p = cmul(p, part[192 + jp * 16 + fr]);
      }
      pw[jp] = cmul(p, cof);
      c[jp] = cmul(cc, cof);
      l3[jp] = cmul(l2[jp], lam[jp]);
      asm volatile("" : "+v"(pw[jp]), "+v"(c[jp]), "+v"(l3[jp]), "+v"(l4[jp]));
#pragma unroll
      for (int i = 0; i < 16; ++i) {
        s[jp][i] = cmul(s[jp][i], cof);
        asm volatile("" : "+v"(s[jp][i]));
      }
    }
    __builtin_amdgcn_s_setprio(3);
    lds_wait<SW_G0E, 0>(U, 4);
    lds_wait<SW_GRP, 1>(U, 4);
    const __amdgpu_buffer_rsrc_t rs = __builtin_amdgcn_make_buffer_rsrc((void*)(ws + WS_HS), 0, 8 << 20, 0x00020000);
    const int hb = ((tm * 2 + wr) * 32 + tn * 2) * 8192 + fq * 2048 + (wc * 32 + fr) * 16;
    const uint32_t hl = lds_base(smem) + G0_STAGE + wr * 8192 + (fq ^ g1_rot(wr)) * 2048 + (wc * 32 + fr) * 16;
#pragma unroll
    for (int jp = 0; jp < 2; ++jp) {
      const int pl = wc * 32 + jp * 16 + fr;
      const cf hin = (grp[192 + pl] + grp[128 + pl]) + (grp[64 + pl] + grp[pl]);
      cf t = cmad(pw[jp], hin, c[jp]);
#pragma unroll
      for (int k = 0; k < 4; ++k) {
        s[jp][4 * k] = cmad(t, lam[jp], s[jp][4 * k]);
        s[jp][4 * k + 1] = cmad(t, l2[jp], s[jp][4 * k + 1]);
        s[jp][4 * k + 2] = cmad(t, l3[jp], s[jp][4 * k + 2]);
        t = cmul(t, l4[jp]);
        s[jp][4 * k + 3] = s[jp][4 * k + 3] + t;
      }
#pragma unroll
      for (int h = 0; h < 2; ++h) {
        u32x4 re, im;
#pragma unroll
        for (int i = 0; i < 4; ++i) {
          re[i] = pk_bf(s[jp][8 * h + 2 * i].x, s[jp][8 * h + 2 * i + 1].x);
          im[i] = pk_bf(-s[jp][8 * h + 2 * i].y, -s[jp][8 * h + 2 * i + 1].y);
        }
        __builtin_amdgcn_raw_buffer_store_b128(re, rs, (hb ^ (h * 64)) + jp * 256 + h * 1024, 0, 16);
        __builtin_amdgcn_raw_buffer_store_b128(im, rs, (hb ^ (h * 64)) + jp * 256 + h * 1024 + 8192, 0, 16);
        sta4((hl ^ (h * 64)) + jp * 256 + h * 1024, re);
        sta4((hl ^ (h * 64)) + jp * 256 + h * 1024 + G0_STAGE, im);
      }
    }
  }
  wait_lgkm0();
  lds_signal<SW_HS>(U);
  if (T.late) __builtin_amdgcn_s_setprio(0);
  publish(ws, 4 * tm + (T.late ? 2 : T.nt >> 2), lane);
  if (T.late) {
    bar();
    bar();
  }
}

__device__ __forceinline__ void gemm1(const Args& A, char* smem, int wave, int lane) {
  char* const ws = A.ws;
  const Tile T = tile_map();
  const int tm = T.tm, nt = T.nt;
  const uint32_t U = 4;
  const int wr = wave >> 1, wc = wave & 1, fr = lane & 15, fq = lane >> 4;
  f32x4 acc[4][4];
  if (T.late) {
    const char* px = reinterpret_cast<const char*>(A.xB + tm * 128 * 1024 + nt * 128 + wc * 64);
    const uint32_t pxo = (wr * 64 + g1_rot(wr) * 16 + fq * 4) * 2048 + fr * 8;
    const u32x2 dw = reinterpret_cast<const u32x2*>(A.Dsk + nt * 128 + wc * 64)[fr];
    u32x2 xw[16];
#pragma unroll
    for (int i = 0; i < 16; ++i) xw[i] = *reinterpret_cast<const u32x2*>(px + ((pxo ^ ((i >> 2) << 15)) + (i & 3) * 2048));
#pragma unroll
    for (int i = 0; i < 32; ++i) {
      const uint32_t a = xw[i >> 1][i & 1], d = dw[i & 1];
      const uint32_t p = pk_bf(u2f(a << 16) * u2f(d << 16), u2f(a & 0xFFFF0000u) * u2f(d & 0xFFFF0000u));
      acc[i >> 3][(i & 1) * 2][(i >> 1) & 3] = u2f(p << 16);
      acc[i >> 3][(i & 1) * 2 + 1][(i >> 1) & 3] = u2f(p & 0xFFFF0000u);
    }
#pragma unroll
    for (int m = 0; m < 4; ++m) asm volatile("" : "+v"(acc[m][0]), "+v"(acc[m][1]), "+v"(acc[m][2]), "+v"(acc[m][3]));
    lds_wait<SW_G0E, 2>(U, 4);
#pragma nounroll
    for (int k = 0; k < 3; ++k) {
      if (k == 2) lds_wait<SW_GRP, 2>(U, 4);
      stage_c(A, wave, lane, k);
    }
    wait_vm<0>();
    lds_signal<SW_CP>(U);
  } else {
#pragma unroll
    for (int i = 0; i < 16; ++i) acc[i >> 2][i & 3] = Z4;
  }
  {
    Core<true> g{acc};
    g.pa0 = lds_base(smem) + G0_STAGE + wr * 8192 + ((fr >> 1) & 1) * 1024 + (8 * fq + (fr >> 2) + (fr & 2) * 2) * 16 + (fr & 1) * 8;
    g.pa1 = g.pa0 ^ 64;
    g.pb0 = lds_base(smem) + G0_STAGE + 16384 + (wc * 64 + fr) * 128 + (fq ^ (fr & 7)) * 16;
    g.pb1 = g.pb0 ^ 64;
#pragma unroll
    for (int m = 0; m < 4; ++m) asm volatile("" : "+v"(acc[m][0]), "+v"(acc[m][1]), "+v"(acc[m][2]), "+v"(acc[m][3]));
    lds_wait<SW_CP, 0>(U, 4);
    lds_wait<SW_HS, 0>(U, 4);
    __builtin_amdgcn_s_setprio(0);
    g.run(0, T.late ? 32 - NX : NX, 32);
    const __amdgpu_buffer_rsrc_t rp = __builtin_amdgcn_make_buffer_rsrc((void*)(ws + WS_PS), 0, 8 << 20, 0x00020000);
    int po = bid_x() * 32768 + wave * 8192 + lane * 16;
    asm volatile("" : "+v"(po));
    auto part = [&](int m) {
      if (T.late) return;
      asm volatile("" : "+v"(acc[m][0]), "+v"(acc[m][1]), "+v"(acc[m][2]), "+v"(acc[m][3]));
#pragma unroll
      for (int i = 0; i < 2; ++i) {
        const f32x4 a = acc[m][2 * i], b = acc[m][2 * i + 1];
        __builtin_amdgcn_raw_buffer_store_b128(u32x4{pk_bf(a[0], b[0]), pk_bf(a[1], b[1]), pk_bf(a[2], b[2]), pk_bf(a[3], b[3])}, rp,
                                               po + (2 * m + i) * 1024, 0, 16);
      }
      __builtin_amdgcn_sched_barrier(0);
    };
#pragma unroll
    for (int i = 0; i < 32; ++i) {
      g.mm(i);
      if (i >= 12 && (i & 7) == 4) part((i >> 3) - 1);
    }
    asm volatile("s_nop 7" : "+v"(acc[3][0]), "+v"(acc[3][1]), "+v"(acc[3][2]), "+v"(acc[3][3]));
    part(3);
  }
  asm volatile("" : "+v"(lane));
  if (!T.late) {
    lds_signal<SW_PL>(U);
    publish(ws, 64 + bid_x(), lane);
    warm_dev(smem, ws + WS_PS, bid_x() * 32768 + wave * 8192 + lane * 128);
  } else {
    const __amdgpu_buffer_rsrc_t ro = __builtin_amdgcn_make_buffer_rsrc((void*)A.out, 0, 16 << 20, 0x00020000);
    const int o = ((tm * 128 + wr * 64 + g1_rot(wr) * 16 + (lane >> 4) * 4) * 2048 + 1024 + nt * 128 + wc * 64 + (lane & 15) * 4) * 4;
    const uint32_t pp = lds_base(smem) + wave * 8192 + lane * 16;
    lds_wait<SW_PL, 0>(U, 4);
    u32x4 p[8];
#pragma unroll
    for (int i = 0; i < 8; ++i) p[i] = ldsa(pp + i * 1024);
#pragma unroll
    for (int m = 0; m < 4; ++m) {
      u32x4 y[4];
#pragma unroll
      for (int i = 0; i < 16; ++i) {
        const int n = i & 3, r = i >> 2;
        const uint32_t w = p[2 * m + (n >> 1)][r];
        y[r][n] = f2u(acc[m][n][r] + u2f((n & 1) ? w & 0xFFFF0000u : w << 16));
      }
      asm volatile("" : "+v"(y[0]), "+v"(y[1]), "+v"(y[2]), "+v"(y[3]));
#pragma unroll
      for (int r = 0; r < 4; ++r) __builtin_amdgcn_raw_buffer_store_b128(y[r], ro, (o ^ (m << 17)) + r * 8192, 0, 16);
      __builtin_amdgcn_sched_barrier(0);
    }
  }
}

}

extern "C" __global__ __launch_bounds__(1024, 4) void k_all(const uint32_t* x2, const unsigned short* w_r, const unsigned short* w_i,
                                                           const unsigned short* xB, const unsigned short* Lre,
                                                           const unsigned short* Lim, const unsigned short* Bre,
                                                           const unsigned short* Bim, const unsigned short* Cre,
                                                           const unsigned short* Cim, const unsigned short* ldt,
                                                           const unsigned short* Dsk, float* out, char* ws) {
  __shared__ __attribute__((aligned(256))) char smem_[LDS_BYTES];
  char* const smem = smem_ + 256;
  const Args A{x2, w_r, w_i, xB, Lre, Lim, Bre, Bim, Cre, Cim, ldt, Dsk, out, ws};
  const int tid = tid_x(), lane = tid & 63;
  int wave = __builtin_amdgcn_readfirstlane(tid >> 6);
  const uint32_t ldsb = lds_base(smem);
  {
    const int o = lane * 128 + (lane >> 5) * ((bid_x() << 9) & 0x7000);
    asm volatile(
        "s_cmp_lg_u32 %1, %3\n\ts_cbranch_scc1 1f\n\ts_getpc_b64 s[90:91]\n\ts_add_u32 s90, s90, k_all@rel32@lo+4\n\t"
        "s_addc_u32 s91, s91, k_all@rel32@hi+12\n\ts_mov_b32 m0, %2\n\ts_nop 0\n\tglobal_load_lds_dword %0, s[90:91]\n1:"
        ::"v"(o < 34176 - 16 ? o : 34176 - 16), "s"(wave), "s"(ldsb + L_SCR2), "n"(0)
        : "s90", "s91", "scc", "memory");
  }
  if (wave < 8) {
    const uint32_t o = (2 * wave + 16 * (lane >> 3)) * 16384 + (bid_x() & 7) * 2048 + ((bid_x() >> 3) & 15) * 128 + ((lane & 7) ^ ((lane >> 5) * 4)) * 16;
    if (bid_x() >= 128)
      asm volatile(
          "s_mov_b32 m0, %4\n\ts_sleep " XSTR(0) "\n\tglobal_load_lds_dwordx4 %0, %2 nt\n\tglobal_load_lds_dwordx4 %1, %2 offset:1024 nt\n\t"
          "global_load_lds_dwordx4 %0, %3 offset:2048 nt\n\tglobal_load_lds_dwordx4 %1, %3 offset:3072 nt"
          ::"v"(o), "v"(o + (2 << 20) - 1024), "s"(x2), "s"(x2 + (16384 - 2048) / 4), "s"(ldsb + 3 * G0_STAGE + wave * 4096)
          : "memory");
    else
      stage0(A, smem, wave & 3, lane, 0, 1 + (wave >> 2));
  } else if (wave < 12) {
    const int wc = (bid_x() >> 3) & 15;
    if (bid_x() >= 128 && (wc & 7) != 3 && wave != 10)
      warm_r(A, smem, lane, wave == 11 ? (wc & 8) | 3 : wc, wave == 11 ? (wc & 7) - ((wc & 7) > 3) : wave - 8);
  } else if (wave == 15) {
    const uint32_t o = tile_map().tn * 128 + (lane & 31) * 4;
    asm volatile(
        "s_mov_b32 m0, %4\n\ts_mov_b32 exec_hi, 0\n\tglobal_load_lds_dword %0, %1\n\ts_add_u32 m0, m0, 128\n\ts_nop 0\n\t"
        "global_load_lds_dword %0, %2\n\ts_add_u32 m0, m0, 128\n\ts_nop 0\n\tglobal_load_lds_dword %0, %3\n\ts_mov_b32 exec_hi, -1"
        ::"v"(o), "s"(Lre), "s"(Lim), "s"(ldt), "s"(ldsb + L_PRM + 512)
        : "memory", "scc");
  } else if (wave == 13) {
    warm1(smem, Dsk, (lane & 15) * 128);
  }
  asm volatile("" : "+s"(wave));
  bool g1 = false;
  if (__builtin_expect(wave >= 12, 0)) {
    if (wave == 12) *(volatile lds_u32*)((lds_u32*)(smem + L_SYNC) + lane) = 0;
    mfma_role(A, smem, wave - 12, lane);
    g1 = bid_x() < 128;
  } else if (__builtin_expect(wave >= 8, 0)) {
    loader_role(A, smem, wave - 8, lane);
    __builtin_amdgcn_s_waitcnt(0x0F70);
  } else {
    g1 = fft_role(A, smem, wave, lane);
    __builtin_amdgcn_s_waitcnt(0x0F70);
  }
  if (g1) gemm1(A, smem, wave & 3, (int)__builtin_amdgcn_mbcnt_hi(~0u, __builtin_amdgcn_mbcnt_lo(~0u, 0u)));
}
#if defined(__HIP_DEVICE_COMPILE__) && __HIP_DEVICE_COMPILE__
#pragma clang attribute pop
#endif

extern "C" void kernel_launch(void* const* d_in, const int* in_sizes, int n_in, void* d_out, int out_size, void* d_ws,
                              size_t ws_size, hipStream_t stream) {
  typedef const unsigned short* P;
  k_all<<<256, 1024, 0, stream>>>((const uint32_t*)d_in[0], (P)d_in[1], (P)d_in[2], (P)d_in[3], (P)d_in[4], (P)d_in[5], (P)d_in[6],
                                  (P)d_in[7], (P)d_in[8], (P)d_in[9], (P)d_in[10], (P)d_in[11], (float*)d_out, (char*)d_ws);
}
```

```cpp
#include <hip/hip_runtime.h>
#include <stdint.h>

namespace {

#if defined(__HIP_DEVICE_COMPILE__) && __HIP_DEVICE_COMPILE__
#pragma clang attribute push(__attribute__((target("no-packed-fp32-ops"))), apply_to = function)
#endif
__device__ __forceinline__ int tid_x() { return __builtin_amdgcn_workitem_id_x(); }
__device__ __forceinline__ int bid_x() { return __builtin_amdgcn_workgroup_id_x(); }

typedef float cf __attribute__((ext_vector_type(2)));
typedef short bf16x8 __attribute__((ext_vector_type(8)));
typedef short s16x4 __attribute__((ext_vector_type(4)));
typedef __bf16 b16x2 __attribute__((ext_vector_type(2)));
typedef float f32x4 __attribute__((ext_vector_type(4)));
typedef uint32_t u32x4 __attribute__((ext_vector_type(4)));
typedef uint32_t u32x2 __attribute__((ext_vector_type(2)));
typedef unsigned short u16;
typedef unsigned long long u64;
typedef __attribute__((address_space(3))) uint32_t lds_u32;

__device__ __forceinline__ float u2f(uint32_t u) { return __builtin_bit_cast(float, u); }
__device__ __forceinline__ uint32_t f2u(float f) { return __builtin_bit_cast(uint32_t, f); }
__device__ __forceinline__ float bf2f(u16 b) { return u2f((uint32_t)b << 16); }
__device__ __forceinline__ float rbf(float f) { return bf2f(__builtin_bit_cast(u16, (__bf16)f)); }
__device__ __forceinline__ uint32_t pk_bf(float a, float b) {
  const b16x2 h = b16x2{(__bf16)a, (__bf16)b};
  return __builtin_bit_cast(uint32_t, h);
}
__device__ __forceinline__ cf fin(cf v) {
  float x = v.x, y = v.y;
  asm("" : "+v"(x), "+v"(y));
  return cf{x, y};
}
__device__ __forceinline__ cf cmul(cf a, cf b) {
  return cf{__builtin_fmaf(a.x, b.x, -(a.y * b.y)), __builtin_fmaf(a.x, b.y, a.y * b.x)};
}
__device__ __forceinline__ cf cmad(cf a, cf b, cf c) {
  return cf{__builtin_fmaf(a.x, b.x, __builtin_fmaf(-a.y, b.y, c.x)), __builtin_fmaf(a.x, b.y, __builtin_fmaf(a.y, b.x, c.y))};
}
__device__ __forceinline__ cf cisr(int k, float inv_n) {
  const float r = (float)k * inv_n;
  return cf{__builtin_amdgcn_cosf(r), -__builtin_amdgcn_sinf(r)};
}
template <int N> __device__ __forceinline__ cf cis(int k) { return cisr(k, 1.0f / N); }
template <int N> __device__ __forceinline__ void wait_vm() { asm volatile("s_waitcnt vmcnt(%0)" ::"n"(N) : "memory"); }
__device__ __forceinline__ void wait_lgkm0() { asm volatile("s_waitcnt lgkmcnt(0)" ::: "memory"); }
__device__ __forceinline__ void glds2_dev(const void* sb, uint32_t v0, uint32_t lds_dst) {
  uint32_t keep;
  asm volatile(
      "s_mov_b32 %0, m0\n\ts_mov_b32 m0, %3\n\ts_nop 0\n\tglobal_load_lds_dwordx4 %1, %2 sc1\n\t"
      "global_load_lds_dwordx4 %1, %2 offset:1024 sc1\n\ts_mov_b32 m0, %0"
      : "=&s"(keep)
      : "v"(v0), "s"(sb), "s"(lds_dst)
      : "memory");
}
__device__ __forceinline__ void gdev2(const void* sb, uint32_t v0, uint32_t v1, uint32_t lds_dst) {
  uint32_t keep;
  asm volatile(
      "s_mov_b32 %0, m0\n\ts_mov_b32 m0, %4\n\ts_nop 0\n\tglobal_load_lds_dwordx4 %1, %3 sc1\n\t"
      "global_load_lds_dwordx4 %2, %3 offset:1024 sc1\n\ts_mov_b32 m0, %0"
      : "=&s"(keep)
      : "v"(v0), "v"(v1), "s"(sb), "s"(lds_dst)
      : "memory");
}
__device__ __forceinline__ void glds4(const void* sb0, const void* sb1, uint32_t v0, uint32_t v1, uint32_t v2, uint32_t v3,
                                      uint32_t lds_dst) {
  uint32_t keep;
  asm volatile(
      "s_mov_b32 %0, m0\n\ts_mov_b32 m0, %7\n\ts_nop 0\n\tglobal_load_lds_dwordx4 %1, %5\n\t"
      "global_load_lds_dwordx4 %2, %5 offset:1024\n\tglobal_load_lds_dwordx4 %3, %6 offset:2048\n\t"
      "global_load_lds_dwordx4 %4, %6 offset:3072\n\ts_mov_b32 m0, %0"
      : "=&s"(keep)
      : "v"(v0), "v"(v1), "v"(v2), "v"(v3), "s"(sb0), "s"(sb1), "s"(lds_dst)
      : "memory");
}
__device__ __forceinline__ constexpr uint32_t lds_base(char*) { return 256; }
__device__ __forceinline__ void bar() {
  __builtin_amdgcn_sched_barrier(0);
  asm volatile("" ::: "memory");
  __builtin_amdgcn_s_barrier();
  asm volatile("" ::: "memory");
  __builtin_amdgcn_sched_barrier(0);
}
__device__ __forceinline__ void bar_lds() {
  asm volatile("s_waitcnt lgkmcnt(0)" ::: "memory");
  bar();
}
template <int W> __device__ __forceinline__ void lds_signal(uint32_t U) {
  asm volatile("s_mov_b64 exec, 1\n\tds_add_u32 %0, %0 offset:%1\n\ts_mov_b64 exec, -1" ::"v"(U), "n"(W * 4 - 4) : "memory");
}
template <int W> __device__ __forceinline__ uint32_t lds_peek(uint32_t U) {
  return __builtin_amdgcn_readfirstlane(*(volatile lds_u32*)(size_t)(U + (W * 4 - 4)));
}
template <int W, int SLEEP> __device__ __forceinline__ void lds_wait(uint32_t U, uint32_t n) {
  uint32_t v, c;
  if (SLEEP)
    asm volatile(
        "s_branch 2f\n1:\n\ts_sleep %5\n2:\n\tds_read_b32 %0, %2 offset:%4\n\ts_waitcnt lgkmcnt(0)\n\tv_readfirstlane_b32 %1, %0\n\t"
        "s_cmp_lt_u32 %1, %3\n\ts_cbranch_scc1 1b"
        : "=&v"(v), "=&s"(c)
        : "v"(U), "s"(n * U), "n"(W * 4 - 4), "n"(SLEEP)
        : "memory", "scc");
  else
    asm volatile(
        "1:\n\tds_read_b32 %0, %2 offset:%4\n\ts_waitcnt lgkmcnt(0)\n\tv_readfirstlane_b32 %1, %0\n\t"
        "s_cmp_lt_u32 %1, %3\n\ts_cbranch_scc1 1b"
        : "=&v"(v), "=&s"(c)
        : "v"(U), "s"(n * U), "n"(W * 4 - 4)
        : "memory", "scc");
}
__device__ __forceinline__ float sel_f(uint64_t m, float a, float b) {
  float r;
  asm("v_cndmask_b32_e64 %0, %1, %2, %3" : "=v"(r) : "v"(b), "v"(a), "s"(m));
  return r;
}
__device__ __forceinline__ cf sel_c(uint64_t m, cf a, cf b) { return cf{sel_f(m, a.x, b.x), sel_f(m, a.y, b.y)}; }
__device__ __forceinline__ u64 ld_dev(const u64* p) { return __hip_atomic_load(p, __ATOMIC_RELAXED, __HIP_MEMORY_SCOPE_AGENT); }
__device__ __forceinline__ void st_dev(u64* p, u64 v) { __hip_atomic_store(p, v, __ATOMIC_RELAXED, __HIP_MEMORY_SCOPE_AGENT); }
__device__ __forceinline__ void st_dev(uint32_t* p, uint32_t v) {
  __hip_atomic_store(p, v, __ATOMIC_RELAXED, __HIP_MEMORY_SCOPE_AGENT);
}
__device__ __forceinline__ f32x4 mfma_b(u32x4 a, u32x4 b, f32x4 c) {
  return __builtin_amdgcn_mfma_f32_16x16x32_bf16(__builtin_bit_cast(bf16x8, a), __builtin_bit_cast(bf16x8, b), c, 0, 0, 0);
}
struct PQ {
  u32x4 p, q;
};
template <int V> struct IC {
  static constexpr int value = V;
};
constexpr f32x4 Z4 = f32x4{0.f, 0.f, 0.f, 0.f};
constexpr float S16 = 0.283235f;
constexpr unsigned SPIN_MAX = 1u << 20;

constexpr size_t WS_EPOCH = 0;
constexpr size_t WS_F1 = 4096;
constexpr size_t WS_P2 = 8192;
constexpr size_t WS_HF = 1u << 19;
constexpr size_t WS_AGG = 1u << 20;
constexpr size_t WS_ZB = 2u << 20;
constexpr size_t WS_ZC = 8u << 20;
constexpr size_t WS_HS = 18u << 20;
constexpr size_t WS_PS = 26u << 20;

constexpr int NX = 14;
constexpr int G0_STAGE = 32768;
constexpr int L_Z = 0;
constexpr int L_X = 131072;
constexpr int L_TB = L_X + 16384;
constexpr int L_WS = L_TB + 4096;
constexpr int L_PART = L_WS + 4096;
constexpr int L_GRP = L_PART + 3584;
constexpr int L_SYNC = -256;
constexpr int L_PRM = L_GRP + 3072;
constexpr int L_SCR2 = L_PRM + 1024;
constexpr int LDS_BYTES = L_SCR2 + 256 + 256;
enum { SW_PART = 1, SW_GRP, SW_PUB, SW_CP, SW_S0, SW_HD, SW_G1, SW_FS, SW_G0E, SW_HS, SW_LD, SW_LD2, SW_F1, SW_PX, SW_FR, SW_PL, SW_P2, SW_HO };

struct Args {
  const uint32_t* x2;
  const u16 *w_r, *w_i, *xB, *Lre, *Lim, *Bre, *Bim, *Cre, *Cim, *ldt, *Dsk;
  float* out;
  char* ws;
};

struct Tile {
  int tm, tn, nt, late;
};
__device__ __forceinline__ Tile tile_map() {
  const int bid = bid_x(), late = bid >> 7, cq = (bid & 1) * 4 + ((bid >> 5) & 3);
  return Tile{(bid >> 1) & 15, late * 8 + cq, cq, late};
}
__device__ __forceinline__ u32x4 lds4(const void* p) { return *reinterpret_cast<const u32x4*>(p); }
__device__ __forceinline__ u32x4 ldsa(uint32_t a) { return *(__attribute__((address_space(3))) u32x4*)(size_t)a; }
__device__ __forceinline__ void lds_cf2(const void* p, cf& a, cf& b) {
  const f32x4 v = *reinterpret_cast<const f32x4*>(p);
  a = cf{v[0], v[1]};
  b = cf{v[2], v[3]};
}
template <int W> __device__ __forceinline__ void group_sync(uint32_t U, uint32_t n) {
  wait_lgkm0();
  lds_signal<W>(U);
  lds_wait<W, 0>(U, n);
}
__device__ __forceinline__ int k_rot() { return (bid_x() & 6) * 2 + (bid_x() & 1) * 8; }
__device__ __forceinline__ void stage0(const Args& A, char* smem, int wave, int lane, int kt, int parts = 3) {
  const Tile T = tile_map();
  const int prow = lane >> 3;
  const uint32_t va = (T.tm * 128 + wave * 32 + prow) * 2048 + ((lane & 7) ^ (prow & 3)) * 16;
  const uint32_t vb = (T.tn * 64 + wave * 16 + prow) * 2048 + ((lane & 7) ^ prow) * 16;
  const uint32_t dst = lds_base(smem) + wave * 4096 + (kt & 3) * G0_STAGE;
  kt = (kt + k_rot()) & 15;
  if (parts & 1) glds4(A.xB + kt * 64, A.xB + kt * 64, va, va + 15360, (va ^ 64) + 2 * 15360, (va ^ 64) + 3 * 15360, dst);
  if (parts & 2) glds4(A.Bre + kt * 64, A.Bim + kt * 64 - 1024, vb, vb + 15360, vb, vb + 15360, dst + 16384);
}
__device__ __forceinline__ int g1_kt(int late, int s) {
  const int c = 2 * tile_map().nt, g = c & 8;
  if (s < 2) return late * 16 + c + s;
  if (s < (late ? 10 : 8)) {
    int j = s - 2;
    if (!late && j >= ((c + 2) & 7)) j += 2;
    return ((j + 6) & 7) + (late ? g ^ 8 : g);
  }
  const int j = late ? s - 10 : s + 14 - NX;
  return 16 + (j < c ? j : j + 2);
}
__device__ __forceinline__ int g1_rot(int wr) { return (tile_map().nt ^ (wr << 1)) & 3; }
__device__ __forceinline__ uint32_t g1_vc(int wave, int lane) {
  return (tile_map().nt * 128 + (wave >> 1) * 64 + (wave & 1) * 2 + (lane >> 3) * 4) * 2048 + ((lane & 7) ^ (lane >> 3)) * 16;
}
__device__ __forceinline__ void stage_c(const Args& A, int wave, int lane, int s) {
  const int kt = g1_kt(tile_map().late, s);
  const u16* src = ((kt & 1) ? A.Cim : A.Cre) + (kt >> 1) * 64;
  const uint32_t vc = g1_vc(wave, lane);
  glds4(src, src, vc, vc + 65536 - 1024, vc, vc + 65536 - 1024, 256 + (s + 1) * G0_STAGE + 16384 + wave * 4096);
}
__device__ __forceinline__ void warm1(char* smem, const void* base, uint32_t off) {
  uint32_t keep;
  asm volatile("s_mov_b32 %0, m0\n\ts_mov_b32 m0, %3\n\ts_nop 0\n\tglobal_load_lds_dword %1, %2\n\ts_mov_b32 m0, %0"
               : "=&s"(keep)
               : "v"(off), "s"(base), "s"(lds_base(smem) + L_SCR2)
               : "memory");
}
__device__ __forceinline__ void warm_dev(char* smem, const void* base, uint32_t off) {
  uint32_t keep;
  asm volatile("s_mov_b32 %0, m0\n\ts_mov_b32 m0, %3\n\ts_nop 0\n\tglobal_load_lds_dword %1, %2 sc1\n\ts_mov_b32 m0, %0"
               : "=&s"(keep)
               : "v"(off), "s"(base), "s"(lds_base(smem) + L_SCR2)
               : "memory");
}
__device__ __forceinline__ void warm_r(const Args& A, char* smem, int lane, int c, int i) {
  const int bid = bid_x(), tm = ((bid >> 1) & 3) + (c & 3) * 4, nt = (bid & 1) * 4 + (c >> 2);
  uint64_t px = (uint64_t)A.xB, pbr = (uint64_t)A.Bre, pbi = (uint64_t)A.Bim;
  asm volatile("" : "+s"(px), "+s"(pbr), "+s"(pbi));
  px += (tm * 128 + (nt & 3) * 32 + (lane & 31)) * 2048;
  const uint64_t pb = ((lane & 16) ? pbi : pbr) + (nt * 64 + (c & 3) * 16 + (lane & 15)) * 2048;
  const int hi = lane >> 5;
  const bool k2 = i == 4 || i == 6;
  const int sl = i < 3 ? i + 1 : (k2 ? i - 4 : i + 1) + hi;
  const uint64_t pfp = ((k2 || (i < 3 && hi)) ? pb + (k2 ? 1 << 20 : 0) : px) + ((sl + k_rot()) & 15) * 128;
  uint32_t keep;
  asm volatile("s_mov_b32 %0, m0\n\ts_mov_b32 m0, %2\n\ts_nop 0\n\tglobal_load_lds_dword %1, off\n\ts_mov_b32 m0, %0"
               : "=&s"(keep)
               : "v"(pfp), "s"(lds_base(smem) + L_SCR2)
               : "memory");
}
__device__ __forceinline__ u32x2 lds_tr(uint32_t a) {
  return __builtin_bit_cast(u32x2, __builtin_amdgcn_ds_read_tr16_b64_v4i16((__attribute__((address_space(3))) s16x4*)(size_t)a));
}
__device__ __forceinline__ void sta4(uint32_t a, u32x4 v) { *(__attribute__((address_space(3))) u32x4*)(size_t)a = v; }
template <int P> __device__ __forceinline__ void prio(bool on) {
  __builtin_amdgcn_sched_barrier(0);
  if (on) __builtin_amdgcn_s_setprio(P);
  __builtin_amdgcn_sched_barrier(0);
}
__device__ __forceinline__ void row_head(u32x4 (&x)[2][2], const PQ& m16, const PQ& m1, const cf (&tw)[2][4], bool on) {
  f32x4 dr[2][2], di[2][2];
#pragma unroll
  for (int k = 0; k < 2; ++k) {
    const f32x4 ur = mfma_b(x[k][0], m16.p, Z4), ui = mfma_b(x[k][0], m16.q, Z4);
    const u32x4 xn = x[k][1] ^ 0x80008000u;
    dr[k][0] = mfma_b(x[k][1], m1.p, ur), di[k][0] = mfma_b(x[k][1], m1.q, ui);
    dr[k][1] = mfma_b(xn, m1.p, ur), di[k][1] = mfma_b(xn, m1.q, ui);
  }
  prio<0>(on);
#pragma unroll
  for (int i = 0; i < 16; ++i) {
    const int k = i >> 3, t = (i >> 2) & 1, r = i & 3;
    const cf v = fin(cmul(cf{dr[k][t][r], di[k][t][r]}, tw[t][r]));
    dr[k][t][r] = v.x, di[k][t][r] = v.y;
  }
  prio<1>(on);
#pragma unroll
  for (int i = 0; i < 16; ++i) x[i >> 3][(i >> 2) & 1][i & 3] = pk_bf(dr[i >> 3][(i >> 2) & 1][i & 3], di[i >> 3][(i >> 2) & 1][i & 3]);
}
template <int W, bool B = false, bool D = false>
__device__ __forceinline__ void poll_count(char* ws, int idx, uint32_t n, uint32_t U, const void* src = nullptr, uint32_t v0 = 0, uint32_t v1 = 0,
                                           uint32_t dst = 0) {
  uint32_t c0, c1, c2, t, lim = SPIN_MAX;
#define ASK(c) "global_atomic_add " c ", %5, %5, %6 sc0\n\t"
#define CHK(c) "s_waitcnt vmcnt(2)\n\tv_readfirstlane_b32 %3, " c "\n\ts_cmp_ge_u32 %3, %7\n\ts_cbranch_scc1 2f\n\t" ASK(c)
  asm volatile(
      "s_mov_b64 exec, 1\n\t" ASK("%0") "s_sleep 4\n\t" ASK("%1") "s_sleep 4\n\t" ASK("%2") ".if %10\n\ts_barrier\n.endif\n1:\n\t" CHK("%0") CHK("%1") CHK("%2")
      "s_sub_u32 %4, %4, 1\n\ts_cbranch_scc0 1b\n2:\n\tds_add_u32 %8, %8 offset:%9\n\ts_mov_b64 exec, -1\n.if %11\n\t"
      "s_mov_b32 %3, m0\n\ts_mov_b32 m0, %15\n\ts_nop 0\n\tglobal_load_lds_dwordx4 %13, %12 sc1\n\tglobal_load_lds_dwordx4 %13, %12 offset:1024 sc1\n\t"
      "global_load_lds_dwordx4 %14, %12 offset:2048 sc1\n\tglobal_load_lds_dwordx4 %14, %12 offset:3072 sc1\n\ts_mov_b32 m0, %3\n\t"
      "s_waitcnt vmcnt(4)\n.else\n\ts_waitcnt vmcnt(0)\n.endif"
      : "=&v"(c0), "=&v"(c1), "=&v"(c2), "=&s"(t), "+&s"(lim)
      : "v"(0), "s"(ws + WS_HF + idx * 128), "s"(n), "v"(U), "n"(W * 4 - 4), "n"(B), "n"(D), "s"(D ? src : ws), "v"(v0), "v"(v1), "s"(dst)
      : "memory", "scc");
#undef ASK
#undef CHK
}
__device__ __forceinline__ bool fft_role(const Args& A, char* smem, int f, int lane) {
  char* const ws = A.ws;
  const Tile T = tile_map();
  const int bid = bid_x(), li = bid & 127;
  int l15 = lane & 15, q = lane >> 4;
  lds_u32* const sw = (lds_u32*)(smem + L_SYNC);
  const uint32_t U = (uint32_t)(size_t)(sw + 1), ldsb = lds_base(smem);
  const __amdgpu_buffer_rsrc_t rzb = __builtin_amdgcn_make_buffer_rsrc((void*)(ws + WS_ZB), 0, 16 << 20, 0x00020000);
  auto fsync = [&](int n) { group_sync<SW_FS>(U, 8 * n); };
  auto bars = [&](int n) {
#pragma nounroll
    for (int i = 0; i < n; ++i) bar();
  };
  u64 epoch = 0;
  if (f == 0) epoch = ld_dev((const u64*)(ws + WS_EPOCH));
  if (f == 7) {
    const __amdgpu_buffer_rsrc_t ra = __builtin_amdgcn_make_buffer_rsrc((void*)(ws + WS_AGG), 0, 512 << 10, 0x00020000);
    __builtin_amdgcn_raw_buffer_store_b128(u32x4{0u, 0u, 0u, 0u}, ra, (T.tm * 2048 + T.tn * 64 + lane) * 16, 0, 16);
    __builtin_amdgcn_raw_buffer_store_b128(u32x4{0u, 0u, 0u, 0u}, ra, (T.tm * 2048 + 1024 + T.tn * 64 + lane) * 16, 0, 16);
    if (!T.late && lane < (T.tn ? 1 : 4)) st_dev((uint32_t*)(ws + WS_HF + (lane ? 4 * T.tm + lane - 1 : 64 + li) * 128), 0u);
    if (lane == 0) st_dev((uint32_t*)(ws + WS_P2) + bid, 0u);
  }
  __builtin_amdgcn_sched_barrier(0);
  auto ks_of = [&](int w) { return bid ? bid + 256 * w : (w < 4 ? 256 * w : 256 * w - 896); };
  {
    const int e = f & 3, n = 4 * q + e;
    uint32_t* tb = reinterpret_cast<uint32_t*>(smem + L_TB) + lane * 4;
    uint32_t* tx = reinterpret_cast<uint32_t*>(smem + L_X) + lane * 4;
    if (f < 4) {
      const cf c = cis<16>(n * l15) * S16;
      tb[e] = pk_bf(c.x, -c.y);
      tb[256 + e] = pk_bf(c.y, c.x);
      if (!T.late) {
        const cf d = cis<32>((2 * n + 1) * l15) * S16;
        tx[e] = pk_bf(d.x, -d.y);
        tx[256 + e] = pk_bf(d.y, d.x);
      }
    }
    if (!T.late) {
      *reinterpret_cast<cf*>(tx + 512 + (f >> 1) * 256 + (f & 1) * 2) = cis<512>(n * (16 * (f >> 2) + l15)) * (1.0f / (64 * S16 * S16));
      const cf w = cis<1024>(l15 + 16 * (f >> 2) + 32 * n);
      *reinterpret_cast<cf*>(tx + 1536 + (f >> 1) * 256 + (f & 1) * 2) = cf{w.y, -w.x};
      const int slot = f < 4 ? l15 : n, u = f < 4 ? q >> 1 : l15 & 1, j = f < 4 ? n & 7 : l15 >> 1;
      const bool own = (bid | (slot & 7)) == 0;
      const cf g = cis<2048>(j * (own ? slot * 128 : ks_of(slot & 7)));
      const int ua = bid ? 0 : (slot >> 2) & 1, ub = bid ? 1 : ua;
      const float al = u == ua ? ((own && f < 4) ? 1.f : 0.5f) : 0.f, be = (u == ub && !own) ? (slot < 8 ? 0.5f : -0.5f) : 0.f;
      const float sa = al + be, da = al - be;
      tx[2560 + (f >> 2) * 512 + e] = pk_bf(sa * g.x, -(f < 4 ? da : sa) * g.y);
      tx[2816 + (f >> 2) * 512 + e] = pk_bf((f < 4 ? sa : da) * g.y, da * g.x);
    }
  }
  if (!T.late) {
    if (f == 0) wait_vm<1>(); else if (f == 7) wait_vm<4>(); else wait_vm<0>();
  }
  bar_lds();
  PQ m16, m1, m8;
  cf tw[2][4];
  u32x4 wv[4];
  m16.p = lds4(smem + L_TB + lane * 16), m16.q = lds4(smem + L_TB + 1024 + lane * 16);
  PQ mk[2];
  auto col_mats = [&]() {
#pragma unroll
    for (int i = 0; i < 8; ++i) {
      const cf c = cis<256>((4 * q + (i & 3)) * (16 * l15 + 2 * f + (i >> 2))) * (1.0f / (16 * S16));
      uint32_t p = pk_bf(c.x, -c.y), o = pk_bf(c.y, c.x);
      asm volatile("" : "+v"(p), "+v"(o));
      mk[i >> 2].p[i & 3] = p, mk[i >> 2].q[i & 3] = o;
    }
    asm volatile("" : "+v"(mk[0].p), "+v"(mk[0].q), "+v"(mk[1].p), "+v"(mk[1].q));
  };
  auto col256 = [&](uint32_t img, int n, bool cj) {
    typedef __attribute__((address_space(3))) u32x2 lds_u32x2;
    const uint32_t own = img + f * 4096 + q * 512, xi = own + (q & 1) * 64 + l15 * 4;
    u32x4 x[4], d[4];
#pragma unroll
    for (int i = 0; i < 16; ++i) x[i >> 2][i & 3] = *(lds_u32*)(size_t)((xi ^ ((i & 4) * 16)) + (i >> 3) * 2048 + (i & 3) * 128);
    wait_lgkm0();
    lds_signal<SW_FS>(U);
#pragma unroll
    for (int i = 0; i < 4; ++i) {
      const f32x4 re = mfma_b(m16.p, x[i], Z4), im = mfma_b(m16.q, x[i], Z4);
      d[i] = u32x4{pk_bf(re[0], im[0]), pk_bf(re[1], im[1]), pk_bf(re[2], im[2]), pk_bf(re[3], im[3])};
    }
    const uint32_t xo = img + q * 8192 + (f >> 1) * 512 + l15 * 16 + (f & 1) * 8;
    asm volatile("" : "+v"(d[0]), "+v"(d[1]), "+v"(d[2]), "+v"(d[3]));
    lds_wait<SW_FS, 0>(U, 8 * n);
#pragma unroll
    for (int i = 0; i < 8; ++i) *(lds_u32x2*)(size_t)(xo + (i & 3) * 2048 + (i >> 2) * 256) = u32x2{d[i >> 2][i & 3], d[2 + (i >> 2)][i & 3]};
    fsync(n + 1);
#pragma unroll
    for (int i = 0; i < 4; ++i) x[i] = ldsa(own + l15 * 16 + (i >> 1) * 2048 + (i & 1) * 256);
    const uint32_t xt = img + f * 4096 + l15 * 128 + (q ^ (l15 & 7)) * 16;
#pragma unroll
    for (int i = 0; i < 4; ++i) {
      const f32x4 re = mfma_b(x[i], mk[i >> 1].p, Z4);
      f32x4 im = mfma_b(x[i], mk[i >> 1].q, Z4);
      if (cj) im = -im;
      d[i] = u32x4{pk_bf(re[0], im[0]), pk_bf(re[1], im[1]), pk_bf(re[2], im[2]), pk_bf(re[3], im[3])};
    }
    asm volatile("" : "+v"(d[0]), "+v"(d[1]), "+v"(d[2]), "+v"(d[3])::"memory");
#pragma unroll
    for (int i = 0; i < 4; ++i) sta4((xt ^ ((i & 1) * 64)) + (i >> 1) * 2048, d[i]);
    asm volatile("" ::: "memory");
  };
  if (T.late) {
    col_mats();
    if (f == 0) wait_vm<1>(); else if (f == 7) wait_vm<3>(); else wait_vm<0>();
    col256(ldsb + 3 * G0_STAGE, 1, false);
    const uint32_t xl = ldsb + 3 * G0_STAGE + f * 4096 + (lane >> 3) * 128 + ((lane ^ (lane >> 3)) & 7) * 16;
#pragma unroll
    for (int i = 0; i < 4; ++i) wv[i] = ldsa(xl + i * 1024);
    const int zo = (2 * f + 16 * (lane >> 3)) * 16384 + (li & 7) * 2048 + (li >> 3) * 128 + (lane & 7) * 16;
    asm volatile("" : "+v"(wv[0]), "+v"(wv[1]), "+v"(wv[2]), "+v"(wv[3]));
#pragma unroll
    for (int i = 0; i < 4; ++i) __builtin_amdgcn_raw_buffer_store_b128(wv[i], rzb, zo + (i >> 1) * 16384 + (i & 1) * (2 << 20), 0, 16);
    bars(2);
  } else {
    const char* tab = smem + L_X + lane * 16;
    m1.p = lds4(tab), m1.q = lds4(tab + 1024), m8.p = lds4(tab + 10240), m8.q = lds4(tab + 11264);
#pragma unroll
    for (int i = 0; i < 4; ++i) lds_cf2(tab + 2048 + i * 1024, tw[i >> 1][(i & 1) * 2], tw[i >> 1][(i & 1) * 2 + 1]);
  }
  wait_vm<0>();
  asm volatile("" : "+v"(epoch));
  const u64 tag = (epoch + 1) | (1ull << 63);
  u64 f1 = tag;
  asm volatile("" : "+v"(f1));
  bar();
  if (f == 0 && lane == 0) st_dev((u64*)(ws + WS_F1) + bid, f1);
  int nb = T.late ? 10 : 11;
  if (T.late && f >= 6) {
    uint64_t pbr = (uint64_t)A.Bre, pbi = (uint64_t)A.Bim;
    asm volatile("" : "+s"(pbr), "+s"(pbi));
    uint64_t pf = ((lane & 16) ? pbi : pbr) + (T.tn * 64 + (T.tm >> 2) * 16 + l15) * 2048 + ((2 * f + (lane >> 5) + k_rot()) & 15) * 128;
    asm volatile("" : "+v"(pf));
    bars(f - 4);
    nb -= f - 4;
    __builtin_amdgcn_s_sleep(5);
    uint32_t keep;
    asm volatile("s_mov_b32 %0, m0\n\ts_mov_b32 m0, %2\n\ts_nop 0\n\tglobal_load_lds_dword %1, off\n\ts_mov_b32 m0, %0"
                 : "=&s"(keep)
                 : "v"(pf), "s"(ldsb + L_SCR2)
                 : "memory");
  }
  cf plam, pcof, lp[11];
  if (f == 2) {
    const u16* raw = reinterpret_cast<const u16*>(smem + L_PRM + 512);
    uint32_t pr = raw[lane], pi = raw[64 + lane], pd = raw[128 + lane];
    asm volatile("" : "+v"(pr), "+v"(pi), "+v"(pd)::"memory");
    auto expf_ = [](float x) {
      const float t = x * 1.44269504088896341f;
      const float r = __builtin_fmaf(x, 1.44269504088896341f, -t) + x * 1.92596299112661746e-8f;
      const float e = __builtin_amdgcn_exp2f(t);
      return __builtin_fmaf(e * r, 0.693147180559945309f, e);
    };
    const float lr = u2f(pr << 16), li = u2f(pi << 16);
    float dt = rbf(expf_(u2f(pd << 16)));
    asm volatile("" : "+v"(dt));
    bar();
    const float zr = dt * lr, zi = dt * li;
    const float e = expf_(zr);
    const float rh = zi * 0.159154936671257019f;
    const float rf = (rh - __builtin_rintf(rh)) + __builtin_fmaf(zi, 6.42063832653e-9f, __builtin_fmaf(zi, 0.159154936671257019f, -rh));
    plam = cf{e * __builtin_amdgcn_cosf(rf), e * __builtin_amdgcn_sinf(rf)};
    asm volatile("" : "+v"(plam));
    bar();
    float den = 2.0f / (lr * lr + li * li);
    asm volatile("" : "+v"(den));
    bar();
    const float nr = plam.x - 1.0f, ni = plam.y;
    pcof = cf{(nr * lr + ni * li) * den, (ni * lr - nr * li) * den};
    lp[0] = cmul(plam, plam);
#pragma unroll
    for (int k = 1; k < 4; ++k) lp[k] = cmul(lp[k - 1], lp[k - 1]);
    asm volatile("" : "+v"(pcof), "+v"(lp[3]));
    bar();
#pragma unroll
    for (int k = 4; k < 10; ++k) lp[k] = cmul(lp[k - 1], lp[k - 1]);
    lp[10] = cmul(lp[8], lp[9]);
    asm volatile("" : "+v"(lp[4]), "+v"(lp[5]), "+v"(lp[6]), "+v"(lp[8]), "+v"(lp[9]), "+v"(lp[10]));
    nb -= 4;
  }
  bars(nb);
  u64 v[4];
  auto poll1 = [&]() {
#pragma unroll
    for (int i = 0; i < 4; ++i) v[i] = ld_dev((const u64*)(ws + WS_F1) + lane + 64 * i);
  };
  if (f == 0) {
    __builtin_amdgcn_s_sleep(5);
    poll1();
  }
  int zo = (q < 2 ? bid : (bid ? 256 - bid : 128)) * 16384 + (q & 1) * 8192 + f * 256 + l15 * 16;
  asm volatile("" : "+v"(zo));
  bar();
  if (T.late && f >= 4) {
    __builtin_amdgcn_s_sleep(6);
    warm1(smem, (f & 1) ? A.Cre : A.Cim, (T.nt * 32 + T.tm * 2 + ((f >> 1) & 1)) * 8192 + lane * 128);
  }
  if (f == 2) {
    if (T.late) lds_wait<SW_LD2, 1>(U, 4);
    cf* prm = reinterpret_cast<cf*>(smem + L_PRM);
    prm[lane] = plam;
    prm[64 + lane] = pcof;
    cf* pt = reinterpret_cast<cf*>(smem + L_PART) + lane;
    pt[64] = lp[3], pt[128] = lp[4], pt[192] = lp[5], pt[384] = lp[6];
    pt = reinterpret_cast<cf*>(smem + L_GRP) + lane;
    pt[0] = cf{1.f, 0.f}, pt[64] = lp[8], pt[128] = lp[9], pt[192] = lp[10];
    wait_lgkm0();
    lds_signal<SW_PART>(U);
    lds_signal<SW_LD2>(U);
  }
  if (f == 0) {
    __builtin_amdgcn_s_setprio(2);
#pragma nounroll
    for (unsigned spins = 0; spins < SPIN_MAX; ++spins) {
      bool ok = true;
#pragma unroll
      for (int i = 0; i < 4; ++i) ok &= v[i] == tag;
      if (__builtin_amdgcn_ballot_w64(!ok) == 0) break;
      __builtin_amdgcn_s_sleep(16);
      poll1();
    }
    lds_signal<SW_F1>(U);
    asm volatile("" ::"v"(v[0]), "v"(v[1]), "v"(v[2]), "v"(v[3]));
  }
  u32x4 zi[4];
  if (!T.late) {
    if (f) lds_wait<SW_F1, 1>(U, 1);
#pragma unroll
    for (int e = 0; e < 4; ++e) zi[e] = __builtin_amdgcn_raw_buffer_load_b128(rzb, zo + e * 2048, 0, 16);
  }
  if (f == 0) __builtin_amdgcn_s_setprio(0);
  if (f == 0 && lane == 0) {
    if (T.late) st_dev((uint32_t*)(ws + WS_P2) + bid, 256u);
    if (bid == 0) st_dev((u64*)(ws + WS_EPOCH), epoch + 1);
  }
  if ((bid | f) == 0) __builtin_amdgcn_s_setprio(3);
  if (!T.late) {
    const __amdgpu_buffer_rsrc_t rr = __builtin_amdgcn_make_buffer_rsrc((void*)A.w_r, 0, 2048 * 1026, 0x00020000);
    const __amdgpu_buffer_rsrc_t ri = __builtin_amdgcn_make_buffer_rsrc((void*)A.w_i, 0, 2048 * 1026, 0x00020000);
    uint32_t sr, si;
    auto wload = [&](int ka) {
      const int oa = ka * 1026, ob = ((2048 - ka) & 2047) * 1026;
      wv[0] = __builtin_amdgcn_raw_buffer_load_b128(rr, lane * 16, oa & ~3, 2);
      wv[1] = __builtin_amdgcn_raw_buffer_load_b128(ri, lane * 16, oa & ~3, 2);
      wv[2] = __builtin_amdgcn_raw_buffer_load_b128(rr, lane * 16, (ob + 2) & ~3, 2);
      wv[3] = __builtin_amdgcn_raw_buffer_load_b128(ri, lane * 16, (ob + 2) & ~3, 2);
      const int so = lane == 0 ? ob : (lane == 1 ? oa + 1024 : ((ka & 1) ? oa + 1022 : ob + 1024));
      sr = __builtin_amdgcn_raw_buffer_load_b16(rr, so, 0, 2);
      si = __builtin_amdgcn_raw_buffer_load_b16(ri, so, 0, 2);
    };
    const bool two = (bid | f) == 0;
    u32x4 w2r, w2i;
    uint32_t s2r, s2i;
    if (two) {
      wload(1024);
      w2r = wv[0], w2i = wv[1], s2r = sr, s2i = si;
    }
    wload(ks_of(f));
    __builtin_amdgcn_sched_barrier(0);
    {
      u32x4 o[4];
      asm volatile("" : "+v"(zi[0]), "+v"(zi[1]), "+v"(zi[2]), "+v"(zi[3]));
#pragma unroll
      for (int c = 0; c < 4; ++c) {
        const u32x4 a = u32x4{zi[0][c], zi[1][c], zi[2][c], zi[3][c]};
        const f32x4 re = mfma_b(a, m8.p, Z4), im = mfma_b(a, m8.q, Z4);
#pragma unroll
        for (int r = 0; r < 4; ++r) o[r][c] = pk_bf(re[r], im[r]);
      }
      const uint32_t oa = l15 * 2048 + f * 256 + ((4 * q) ^ (l15 & 7) ^ ((f & 2) << 1)) * 16;
#pragma unroll
      for (int r = 0; r < 4; ++r) sta4(ldsb + L_Z + (oa ^ (r * 16)), o[r]);
      wait_lgkm0();
      lds_signal<SW_FS>(U);
    }
    uint32_t wsp = 0;
    if (two) {
      sta4(ldsb + L_WS + lane * 16, w2r), sta4(ldsb + L_WS + 1024 + lane * 16, w2i);
      wsp = s2r | s2i << 16;
    }
    asm volatile("" : "+v"(wsp));
    lds_wait<SW_FS, 0>(U, 8);
    prio<1>(!two);
    const uint32_t row = ldsb + L_Z + f * 2048;
    u32x4 x[2][2];
    auto rows = [&]() {
      const uint32_t ra = row + q * 512 + ((l15 >> 2) ^ f ^ ((q & 1) << 2)) * 16 + (l15 & 3) * 4;
#pragma unroll
      for (int i = 0; i < 16; ++i) x[i >> 3][(i >> 2) & 1][i & 3] = *(lds_u32*)(size_t)((ra ^ ((i & 4) * 16)) + (i & 3) * 128 + (i >> 3) * 16384);
    };
    rows();
    if (two) x[1][0] = x[0][0] & 0xFFFF0000u, x[1][1] = x[0][1] & 0xFFFF0000u, x[0][0] &= 0xFFFFu, x[0][1] &= 0xFFFFu;
    asm volatile("" : "+v"(x[0][0]), "+v"(x[0][1]), "+v"(x[1][0]), "+v"(x[1][1])::"memory");
    row_head(x, m16, m1, tw, !two);
    wsp = (uint32_t)__builtin_amdgcn_update_dpp((int)(sr | si << 16), (int)wsp, 0x114, 0xF, 0x2, false);
    sta4(row + lane * 16, wv[0]);
    sta4(row + 1024 + lane * 16, wv[1]);
    if (!two) sta4(row + 16384 + lane * 16, wv[2]), sta4(row + 17408 + lane * 16, wv[3]);
    uint32_t wa = row + (bid & 1) * 2 + l15 * 2 + q * 256, wb = row + (two ? 0 : 16384) + 800 - (bid & 1) * 2 - l15 * 2 - q * 256;
    u32x4 w[4] = {u32x4{0u, 0u, 0u, 0u}, u32x4{0u, 0u, 0u, 0u}, u32x4{0u, 0u, 0u, 0u}, u32x4{0u, 0u, 0u, 0u}};
    asm volatile("" : "+v"(wsp), "+v"(w[0]), "+v"(w[1]), "+v"(w[2]), "+v"(w[3])::"memory");
    auto wread = [&](auto tc) {
      constexpr int t = decltype(tc)::value;
#pragma unroll
      for (int r = 0; r < 4; ++r) {
        uint32_t a = w[0][r], b = w[1][r], c = w[2][r], d = w[3][r];
        asm volatile("ds_read_u16_d16_hi %0, %4 offset:%6\n\tds_read_u16_d16_hi %1, %4 offset:%7\n\t"
                     "ds_read_u16_d16_hi %2, %5 offset:%8\n\tds_read_u16_d16_hi %3, %5 offset:%9"
                     : "+v"(a), "+v"(b), "+v"(c), "+v"(d)
                     : "v"(wa), "v"(wb), "n"(32 * t + 64 * r), "n"(32 * t + 64 * r + 1024), "n"(224 - 32 * t - 64 * r), "n"(1248 - 32 * t - 64 * r)
                     : "memory");
        w[0][r] = a, w[1][r] = b, w[2][r] = c, w[3][r] = d;
      }
    };
#pragma nounroll
    for (int jb = 0;; jb = 4) {
      wread(IC<0>{});
      const uint32_t s0 = __builtin_amdgcn_readlane(wsp, jb), s1 = __builtin_amdgcn_readlane(wsp, jb + 1);
      const uint32_t s2 = two ? s1 : __builtin_amdgcn_readlane(wsp, 63);
#pragma unroll
      for (int t = 0; t < 2; ++t) {
        cf u[4];
        lds_cf2(smem + L_X + 6144 + t * 2048 + lane * 16, u[0], u[1]), lds_cf2(smem + L_X + 7168 + t * 2048 + lane * 16, u[2], u[3]);
        f32x4 ar = mfma_b(m16.p, x[0][t], Z4), ai = mfma_b(m16.q, x[0][t], Z4), br = mfma_b(m16.p, x[1][t], Z4), bi = mfma_b(m16.q, x[1][t], Z4);
        auto half = [](uint32_t w, int hi) { return u2f(hi ? w & 0xFFFF0000u : w << 16); };
        __builtin_amdgcn_sched_barrier(0);
        asm volatile("s_waitcnt lgkmcnt(0)" : "+v"(w[0]), "+v"(w[1]), "+v"(w[2]), "+v"(w[3]), "+v"(u[0]), "+v"(u[1]), "+v"(u[2]), "+v"(u[3]));
        prio<0>(!two);
#pragma unroll
        for (int r = 0; r < 4; ++r) {
          cf fa = cf{u2f(w[0][r]), u2f(w[1][r])}, fb = cf{u2f(w[2][r]), -u2f(w[3][r])};
          if (t == 0 && r == 0 && lane == 0) {
            if (!(bid & 1)) fb = cf{half(s2, 0), -half(s2, 1)};
            fa = (fa + cf{half(s0, 0), -half(s0, 1)}) * 0.5f;
            fb = (fb + cf{half(s1, 0), half(s1, 1)}) * 0.5f;
          }
          if (t == 1 && r == 3 && (bid & 1) && lane == 63) fa = cf{half(s2, 0), half(s2, 1)};
          const cf X1 = cmad(u[r], cf{br[r], bi[r]}, cf{ar[r], ai[r]});
          const cf X2 = cf{__builtin_fmaf(2.f, ar[r], -X1.x), __builtin_fmaf(2.f, ai[r], -X1.y)};
          const cf Yb = cmul(X2, fb), S = fin(cmad(X1, fa, Yb));
          const cf V = fin(cmul(cf{u[r].x, -u[r].y}, cf{__builtin_fmaf(-2.f, Yb.x, S.x), __builtin_fmaf(-2.f, Yb.y, S.y)}));
          ar[r] = S.x, ai[r] = S.y, br[r] = V.x, bi[r] = V.y;
        }
        prio<1>(!two);
        if (t == 0) wread(IC<1>{});
        if (two) ar += br, ai += bi;
#pragma unroll
        for (int r = 0; r < 4; ++r) x[0][t][r] = pk_bf(ar[r], -ai[r]), x[1][t][r] = pk_bf(br[r], -bi[r]);
      }
      if (!two) break;
      if (jb) {
        x[1][0] = x[0][0], x[1][1] = x[0][1];
        x[0][0] = ldsa(row + lane * 16), x[0][1] = ldsa(row + 1024 + lane * 16);
        break;
      }
      sta4(row + lane * 16, x[0][0]), sta4(row + 1024 + lane * 16, x[0][1]);
      rows();
      x[0][0] = x[1][0] & 0xFFFFu, x[0][1] = x[1][1] & 0xFFFFu, x[1][0] &= 0xFFFF0000u, x[1][1] &= 0xFFFF0000u;
      row_head(x, m16, m1, tw, false);
      wa += L_WS - L_Z, wb += L_WS - L_Z;
    }
    row_head(x, m16, m1, tw, !two);
    const uint32_t oa = row + l15 * 128 + (q ^ (l15 & 7)) * 16;
#pragma unroll
    for (int i = 0; i < 4; ++i) {
      const f32x4 re = mfma_b(x[i >> 1][i & 1], m16.p, Z4), im = mfma_b(x[i >> 1][i & 1], m16.q, Z4);
      sta4((oa ^ ((i & 1) * 64)) + (i >> 1) * 16384, u32x4{pk_bf(re[0], im[0]), pk_bf(re[1], im[1]), pk_bf(re[2], im[2]), pk_bf(re[3], im[3])});
    }
    m8.p = lds4(smem + L_X + 12288 + lane * 16), m8.q = lds4(smem + L_X + 13312 + lane * 16);
    fsync(2);
    u32x4 yi[4];
#pragma unroll
    for (int e = 0; e < 4; ++e) yi[e] = lds4(smem + L_Z + (4 * q + e) * 2048 + f * 256 + (l15 ^ ((2 * f + (l15 >> 3)) & 7)) * 16);
    asm volatile("" : "+v"(yi[0]), "+v"(yi[1]), "+v"(yi[2]), "+v"(yi[3]));
    u32x4 o[4];
#pragma unroll
    for (int c = 0; c < 4; ++c) {
      const u32x4 y = u32x4{yi[0][c], yi[1][c], yi[2][c], yi[3][c]};
      const f32x4 re = mfma_b(m8.p, y, Z4), im = mfma_b(m8.q, y, Z4);
#pragma unroll
      for (int r = 0; r < 4; ++r) o[r][c] = pk_bf(re[r], im[r]);
    }
    const int zc = (int)(WS_ZC - WS_ZB) + q * 4096 + f * 256 + l15 * 16;
    asm volatile("" : "+v"(o[0]), "+v"(o[1]), "+v"(o[2]), "+v"(o[3]));
    __builtin_amdgcn_s_setprio(0);
#pragma unroll
    for (int i = 0; i < 4; ++i)
      __builtin_amdgcn_raw_buffer_store_b128(o[i], rzb, zc + ((i & 1) ? (bid ? 256 - bid : 128) : bid) * 16384 + (i >> 1) * 2048, 0, 16);
  } else {
    if (f >= 4) return true;
    if (f == 1) {
      lds_wait<SW_PUB, 4>(U, 4);
      lds_wait<SW_F1, 4>(U, 1);
      __builtin_amdgcn_s_sleep(10);
      poll_count<SW_G1>(ws, 4 * T.tm + 1 - (T.nt >> 2), 4, U);
    }
  }
  const int N = T.late ? 32 - NX : NX;
  if (T.late && f != 1) return false;
  bars(2);
  wait_vm<0>();
  bar();
  if (f == 1) {
    if (T.late) bars(2);
    {
      uint32_t c;
      asm volatile("s_mov_b64 exec, 1\n\tglobal_atomic_add %0, %1, %1, %2 sc0\n\ts_mov_b64 exec, -1\n\ts_barrier\n\ts_waitcnt vmcnt(0)"
                   : "=&v"(c)
                   : "v"(0), "s"(ws + WS_HF + (4 * T.tm + 2) * 128)
                   : "memory");
      if (__builtin_amdgcn_readfirstlane(c) < 8u) poll_count<SW_FR>(ws, 4 * T.tm + 2, 8, U);
    }
    if (T.late) {
      bars(N - 11);
      poll_count<SW_PX, true>(ws, 64 + li, 4, U);
      return false;
    }
    bars(NX - 8);
  } else if (f == 0) {
    if (lane == 0) st_dev((uint32_t*)(ws + WS_P2) + bid, 256u);
    bars(NX - 9);
    const __amdgpu_buffer_rsrc_t rp = __builtin_amdgcn_make_buffer_rsrc((void*)(ws + WS_P2), 0, 1024, 0x00020000);
    u32x4 pw = __builtin_amdgcn_raw_buffer_load_b128(rp, lane * 16, 0, 16);
    bars(2);
#pragma nounroll
    for (unsigned spins = 0; spins < SPIN_MAX; ++spins) {
      if (__builtin_amdgcn_ballot_w64((pw[0] & pw[1] & pw[2] & pw[3] & 256u) == 0) == 0) break;
      __builtin_amdgcn_s_sleep(8);
      pw = __builtin_amdgcn_raw_buffer_load_b128(rp, lane * 16, 0, 16);
      asm volatile("" ::: "memory");
    }
    lds_signal<SW_P2>(U);
    asm volatile("" ::"v"(pw));
    if (lane < 2) st_dev((u64*)(ws + WS_F1) + bid + lane * 128, 0ull);
  } else {
    bars(NX - 7);
  }
  {
    asm volatile("" : "+v"(lane));
    l15 = lane & 15;
    q = lane >> 4;
    constexpr uint32_t img = 256;
    const uint32_t vo = (2 * f + 16 * (lane >> 3)) * 16384 + (li & 7) * 2048 + (li >> 3) * 128 + ((lane & 7) ^ ((lane >> 5) * 4)) * 16;
    if (f) lds_wait<SW_P2, 1>(U, 1);
    gdev2(ws + WS_ZC, vo, vo + (2 << 20) - 1024, img + f * 4096);
    gdev2(ws + WS_ZC + 16384, vo, vo + (2 << 20) - 1024, img + f * 4096 + 2048);
    col_mats();
    wait_vm<0>();
    col256(img, 3, true);
    typedef __attribute__((address_space(3))) u32x2 lds_u32x2;
    const uint32_t xr = img + f * 4096 + q * 128 + ((l15 >> 1) ^ q) * 16 + (l15 & 1) * 8;
    u32x2 z[8];
#pragma unroll
    for (int i = 0; i < 8; ++i) z[i] = *(lds_u32x2*)(size_t)((xr ^ ((i & 1) * 64)) + i * 512);
    const __amdgpu_buffer_rsrc_t ro = __builtin_amdgcn_make_buffer_rsrc((void*)A.out, 0, 16 << 20, 0x00020000);
    const int oo = (2 * f + 16 * q) * 65536 + (li & 7) * 8192 + (li >> 3) * 256 + l15 * 16;
    lds_wait<SW_PL, 2>(U, 4);
#pragma unroll
    for (int i = 0; i < 8; ++i) {
      const uint32_t p0 = z[i][0], p1 = z[i][1];
      __builtin_amdgcn_raw_buffer_store_b128(u32x4{p0 << 16, p0 & 0xFFFF0000u, p1 << 16, p1 & 0xFFFF0000u}, ro, oo + (i >> 2) * 65536 + (i & 3) * (4 << 20), 0, 16);
    }
  }
  return false;
}

__device__ __forceinline__ void publish(char* ws, int idx, int lane) {
  wait_vm<0>();
  if (lane == 0) __hip_atomic_fetch_add((uint32_t*)(ws + WS_HF + idx * 128), 1u, __ATOMIC_RELAXED, __HIP_MEMORY_SCOPE_AGENT);
}
template <int N> __device__ __forceinline__ void publish8(char* ws, int idx) {
  uint32_t v, c;
  asm volatile(
      "s_waitcnt vmcnt(%5)\n\ts_mov_b64 exec, 1\n\tds_add_rtn_u32 %0, %2, %2 offset:%4\n\ts_waitcnt lgkmcnt(0)\n\tv_readfirstlane_b32 %1, %0\n\t"
      "s_cmp_lg_u32 %1, 28\n\ts_cbranch_scc1 1f\n\tv_mov_b32 %0, 1\n\tglobal_atomic_add %2, %0, %3 offset:-4\n1:\n\ts_mov_b64 exec, -1"
      : "=&v"(v), "=&s"(c)
      : "v"(4), "s"(ws + WS_HF + idx * 128), "n"(SW_HD * 4 - 4), "n"(N)
      : "memory", "scc");
}
__device__ __forceinline__ void states(char* ws, char* smem, int wave, int lane, int jp, cf (&v)[16], cf pw, cf lam) {
  const Tile T = tile_map();
  const int wr = wave >> 1, pl = (wave & 1) * 32 + jp * 16 + (lane & 15), fq = lane >> 4;
  const __amdgpu_buffer_rsrc_t rs = __builtin_amdgcn_make_buffer_rsrc((void*)(ws + WS_HS), 0, 8 << 20, 0x00020000);
  int hb = ((T.tm * 2 + wr) * 32 + T.tn * 2) * 8192 + fq * 2048 + pl * 16, hc = (hb ^ 64) + 1024;
  uint32_t hl = lds_base(smem) + G0_STAGE + wr * 8192 + (fq ^ g1_rot(wr)) * 2048 + pl * 16, hm = (hl ^ 64) + 1024;
  uint32_t ga = lds_base(smem) + L_GRP + pl * 8;
  auto grp = [&](int i) { return *(__attribute__((address_space(3))) cf*)(size_t)(ga + i * 512); };
  int hb2 = hb + 8192, hc2 = hc + 8192;
  asm volatile("" : "+v"(hb), "+v"(hc), "+v"(hb2), "+v"(hc2), "+v"(hl), "+v"(hm), "+v"(ga));
  lds_wait<SW_GRP, 1>(4, 4);
  cf h = cmad(pw, (grp(3) + grp(2)) + (grp(1) + grp(0)), v[0]);
#pragma unroll
  for (int k = 0; k < 2; ++k) {
    u32x4 re, im;
#pragma unroll
    for (int i = 0; i < 4; ++i) {
      if (k | i) h = cmad(h, lam, v[8 * k + 2 * i]);
      const cf g = cmad(h, lam, v[8 * k + 2 * i + 1]);
      re[i] = pk_bf(h.x, g.x), im[i] = pk_bf(-h.y, -g.y);
      h = g;
    }
    __builtin_amdgcn_raw_buffer_store_b128(re, rs, k ? hc : hb, 0, 16);
    __builtin_amdgcn_raw_buffer_store_b128(im, rs, k ? hc2 : hb2, 0, 16);
    sta4(k ? hm : hl, re);
    sta4((k ? hm : hl) + G0_STAGE, im);
    __builtin_amdgcn_sched_barrier(0);
  }
  wait_lgkm0();
  lds_signal<SW_HS>(4);
}
__device__ __forceinline__ void loader_role(const Args& A, char* smem, int wave, int lane) {
  char* const ws = A.ws;
  const Tile T = tile_map();
  const int tm = T.tm, tn = T.tn;
  lds_u32* const sw = (lds_u32*)(smem + L_SYNC);
  const uint32_t U = (uint32_t)(size_t)(sw + 1);
  const uint32_t ldsb = lds_base(smem), lds0 = ldsb + wave * 4096;
  const int prow = lane >> 3;
  {
    __builtin_amdgcn_s_setprio(0);
    const uint32_t va = (tm * 128 + wave * 32 + prow) * 2048 + ((lane & 7) ^ (prow & 3)) * 16;
    const uint32_t vb = (tn * 64 + wave * 16 + prow) * 2048 + ((lane & 7) ^ prow) * 16;
    if (T.late) {
      bar();
      const int wc = (bid_x() >> 3) & 15;
      if ((wc & 7) != 3) {
#pragma nounroll
        for (int i = 2 + wave; i < 7; i += 4) warm_r(A, smem, lane, wc, i);
      }
      stage0(A, smem, wave, lane, 0);
    }
    stage0(A, smem, wave, lane, 1);
    {
      uint32_t k = 2, o = ((2 + k_rot()) & 15) * 128, so = 2 * G0_STAGE;
      const u64 bx = (u64)A.xB, br = (u64)A.Bre, bi = (u64)A.Bim - 2048;
      asm volatile(
          "s_add_u32 s88, %[xl], %[o]\n\ts_addc_u32 s89, %[xh], 0\n\ts_add_u32 m0, %[l0], %[so]\n1:\n\ts_waitcnt vmcnt(8)\n\ts_barrier\n\t"
          "global_load_lds_dwordx4 %[a0], s[88:89]\n\tglobal_load_lds_dwordx4 %[a1], s[88:89] offset:1024\n\t"
          "global_load_lds_dwordx4 %[a2], s[88:89] offset:2048\n\tglobal_load_lds_dwordx4 %[a3], s[88:89] offset:3072\n\t"
          "s_add_u32 m0, m0, 16384\n\ts_add_u32 s84, %[brl], %[o]\n\ts_addc_u32 s85, %[brh], 0\n\t"
          "s_add_u32 s86, %[bil], %[o]\n\ts_addc_u32 s87, %[bih], 0\n\ts_add_u32 %[o], %[o], 128\n\ts_and_b32 %[o], %[o], 0x780\n\t"
          "s_add_u32 s88, %[xl], %[o]\n\ts_addc_u32 s89, %[xh], 0\n\t"
          "s_add_u32 %[so], %[so], 0x8000\n\ts_and_b32 %[so], %[so], 0x18000\n\ts_add_u32 %[k], %[k], 1\n\t"
          "global_load_lds_dwordx4 %[b0], s[84:85]\n\tglobal_load_lds_dwordx4 %[b1], s[84:85] offset:1024\n\t"
          "global_load_lds_dwordx4 %[b0], s[86:87] offset:2048\n\tglobal_load_lds_dwordx4 %[b1], s[86:87] offset:3072\n\t"
          "s_add_u32 m0, %[l0], %[so]\n\ts_cmp_lt_u32 %[k], 16\n\ts_cbranch_scc1 1b"
          : [k] "+&s"(k), [o] "+&s"(o), [so] "+&s"(so)
          : [a0] "v"(va), [a1] "v"(va + 15360), [a2] "v"((va ^ 64) + 2 * 15360), [a3] "v"((va ^ 64) + 3 * 15360), [b0] "v"(vb), [b1] "v"(vb + 15360),
            [xl] "s"((uint32_t)bx), [xh] "s"((uint32_t)(bx >> 32)), [brl] "s"((uint32_t)br), [brh] "s"((uint32_t)(br >> 32)),
            [bil] "s"((uint32_t)bi), [bih] "s"((uint32_t)(bi >> 32)), [l0] "s"(lds0)
          : "memory", "scc", "s84", "s85", "s86", "s87", "s88", "s89"
      );
    }
    wait_vm<8>();
    lds_signal<SW_LD>(U);
    wait_vm<0>();
    lds_signal<SW_LD2>(U);
  }
  __builtin_amdgcn_s_setprio(2);
  {
    const int nseg = 2 * tm - 8 * wave;
    cf G = cf{0.f, 0.f};
    lds_wait<SW_PART, 2>(U, 1);
    lds_wait<SW_F1, 2>(U, 1);
    if (nseg > 0) {
      const __amdgpu_buffer_rsrc_t ra = __builtin_amdgcn_make_buffer_rsrc((void*)(ws + WS_AGG), 0, 512 << 10, 0x00020000);
      int o[8];
#pragma unroll
      for (int k = 0; k < 8; ++k) {
        o[k] = ((nseg - 1 - k) * 1024 + tn * 64 + lane) * 16;
        asm volatile("" : "+v"(o[k]));
      }
      u32x4 u[8];
      cf l64 = reinterpret_cast<const cf*>(smem + L_PART)[192 + lane], l128 = reinterpret_cast<const cf*>(smem + L_PART)[384 + lane];
      cf lw = reinterpret_cast<const cf*>(smem + L_GRP)[wave * 64 + lane];
      asm volatile("" : "+v"(l64), "+v"(l128), "+v"(lw));
      lds_wait<SW_PUB, 1>(U, 4);
      __builtin_amdgcn_s_sleep(10);
#pragma nounroll
      for (unsigned spins = 0; spins < SPIN_MAX; ++spins) {
        uint32_t n = 0;
#pragma unroll
        for (int k = 0; k < 8; ++k) u[k] = __builtin_amdgcn_raw_buffer_load_b128(ra, o[k], 0, 16);
#pragma unroll
        for (int k = 0; k < 8; ++k) n += u[k][1] + u[k][3];
        if (__builtin_amdgcn_ballot_w64(n != 2u * (nseg < 8 ? nseg : 8)) == 0) break;
        __builtin_amdgcn_s_sleep(4);
        asm volatile("" ::: "memory");
      }
#pragma unroll
      for (int k = 6; k >= 0; k -= 2)
        G = cmad(G, l128, cmad(cf{u2f(u[k + 1][0]), u2f(u[k + 1][2])}, l64, cf{u2f(u[k][0]), u2f(u[k][2])}));
      G = cmul(G, lw);
    }
    reinterpret_cast<cf*>(smem + L_GRP)[wave * 64 + lane] = G;
    wait_lgkm0();
    lds_signal<SW_GRP>(U);
  }
  int tv = g1_kt(T.late, lane & 31) & 31;
  asm volatile("" : "+v"(tv));
  {
    lds_wait<SW_HO, 1>(U, 4);
    cf v[16];
#pragma unroll
    for (int i = 0; i < 8; ++i) lds_cf2(smem + (T.late ? 0 : 3 * G0_STAGE) + wave * 8192 + i * 1024 + lane * 16, v[2 * i], v[2 * i + 1]);
    cf pw = *reinterpret_cast<const cf*>(smem + L_TB + 2048 + wave * 512 + lane * 8);
    cf lam = reinterpret_cast<const cf*>(smem + L_PRM)[(wave & 1) * 32 + 16 + (lane & 15)];
#pragma unroll
    for (int i = 0; i < 16; i += 4) asm volatile("" : "+v"(v[i]), "+v"(v[i + 1]), "+v"(v[i + 2]), "+v"(v[i + 3]));
    asm volatile("" : "+v"(pw), "+v"(lam));
    __builtin_amdgcn_s_setprio(3);
    states(ws, smem, wave, lane, 1, v, pw, lam);
    __builtin_amdgcn_s_setprio(2);
  }
  const char* ab = ws + WS_HS - 2048 + (size_t)tm * (64 * 8192);
  const uint32_t vh2 = ((wave >> 1) * 262144 + (wave & 1) * 4096 + 2048 + lane * 16) ^ (g1_rot(wave >> 1) * 2048), vh = (vh2 ^ 2048) + 2048;
  auto h2 = [&]() {
    const char* pa = ab + g1_kt(T.late, 2) * 8192;
    glds2_dev(pa, vh, lds0 + 3 * G0_STAGE);
    glds2_dev(pa + 2048, vh2, lds0 + 3 * G0_STAGE + 2048);
  };
  bool got = false;
  if (!T.late) {
    stage_c(A, wave, lane, 0);
    stage_c(A, wave, lane, 1);
    publish8<8>(ws, 4 * tm + (T.nt >> 2));
    wait_vm<0>();
    lds_signal<SW_CP>(U);
    if (wave == 0) poll_count<SW_G1, false, true>(ws, 4 * tm + (T.nt >> 2), 4, U, ab + g1_kt(0, 2) * 8192, vh, vh2, lds0 + 3 * G0_STAGE);
    else lds_wait<SW_HS, 1>(U, 8);
    got = wave == 0;
    stage_c(A, wave, lane, 2);
  } else {
    got = lds_peek<SW_G1>(U) != 0;
    if (got) {
      h2();
      publish8<4>(ws, 4 * tm + 2);
    } else {
      publish8<0>(ws, 4 * tm + 2);
    }
  }
  if (!got) {
    lds_wait<SW_G1, 1>(U, 1);
    h2();
  }
  __builtin_amdgcn_s_setprio(0);
  const int N = T.late ? 32 - NX : NX;
  const uint32_t vc = g1_vc(wave, lane);
  lds_wait<SW_S0, 1>(U, 4);
  {
    uint32_t k = 3, kt = g1_kt(T.late, 3), dst = lds0 + G0_STAGE, t;
    asm volatile(
        "s_lshl_b32 %[t], %[kt], 13\n\ts_add_u32 s86, %[al], %[t]\n\ts_addc_u32 s87, %[ah], 0\n\t"
        "s_mov_b32 m0, %[dst]\n\ts_branch 2f\n1:\n\ts_waitcnt vmcnt(8)\n\ts_barrier\n2:\n\t"
        "global_load_lds_dwordx4 %[h0], s[86:87] sc1\n\tglobal_load_lds_dwordx4 %[h0], s[86:87] offset:1024 sc1\n\t"
        "global_load_lds_dwordx4 %[h1], s[86:87] offset:2048 sc1\n\tglobal_load_lds_dwordx4 %[h1], s[86:87] offset:3072 sc1\n\t"
        "s_add_u32 m0, m0, 16384\n\ts_bitcmp0_b32 %[kt], 0\n\ts_cselect_b64 s[84:85], %[cr], %[ci]\n\ts_lshl_b32 %[t], %[kt], 6\n\t"
        "s_and_b32 %[t], %[t], 0x780\n\ts_add_u32 s84, s84, %[t]\n\ts_addc_u32 s85, s85, 0\n\ts_add_u32 %[k], %[k], 1\n\t"
        "v_readlane_b32 %[kt], %[tv], %[k]\n\ts_add_u32 %[t], %[dst], 0x8000\n\ts_cmp_eq_u32 %[dst], %[dl]\n\ts_cselect_b32 %[dst], %[df], %[t]\n\t"
        "s_lshl_b32 %[t], %[kt], 13\n\ts_add_u32 s86, %[al], %[t]\n\ts_addc_u32 s87, %[ah], 0\n\t"
        "global_load_lds_dwordx4 %[c0], s[84:85]\n\tglobal_load_lds_dwordx4 %[c1], s[84:85] offset:1024\n\t"
        "global_load_lds_dwordx4 %[c0], s[84:85] offset:2048\n\tglobal_load_lds_dwordx4 %[c1], s[84:85] offset:3072\n\t"
        "s_mov_b32 m0, %[dst]\n\ts_cmp_lt_u32 %[k], %[n]\n\ts_cbranch_scc1 1b"
        : [k] "+&s"(k), [kt] "+&s"(kt), [dst] "+&s"(dst), [t] "=&s"(t)
        : [h0] "v"(vh), [h1] "v"(vh2), [c0] "v"(vc), [c1] "v"(vc + 65536 - 1024), [tv] "v"(tv), [cr] "s"(A.Cre), [ci] "s"(A.Cim),
          [al] "s"((uint32_t)(u64)ab), [ah] "s"((uint32_t)((u64)ab >> 32)), [df] "s"(lds0 + G0_STAGE), [dl] "s"(lds0 + 3 * G0_STAGE), [n] "s"(N)
        : "memory", "scc", "s84", "s85", "s86", "s87"
    );
  }
  wait_vm<8>();
  lds_signal<SW_LD>(U);
  wait_vm<0>();
  lds_signal<SW_LD2>(U);
  if (T.late) {
    const char* ps = ws + WS_PS + (size_t)(bid_x() & 127) * 32768 + wave * 8192;
    lds_wait<SW_PX, 1>(U, 1);
#pragma nounroll
    for (int i = 0; i < 4; ++i) glds2_dev(ps + i * 2048, lane * 16, ldsb + wave * 8192 + i * 2048);
    wait_vm<0>();
    lds_signal<SW_PL>(U);
  }
}

template <bool TR> struct Core {
  struct AF {
    u32x2 l, h;
  };
  f32x4 (&acc)[4][4];
  uint32_t pa0, pa1, pb0, pb1;
  AF a[2][2];
  int so = 0;
  u32x4 b[4][2];
  static constexpr int DA = TR ? 2048 : 512;
  static constexpr int pn(int i) { return (i >> 3) == 1 ? 3 - ((i >> 1) & 3) : (i >> 1) & 3; }
  static constexpr int pk(int i) { return (i ^ (i >> 1)) & 1; }
  __device__ __forceinline__ void ra(int m, int j) {
    AF& d = a[m & 1][j >> 1];
    if (TR) {
      if (j & 1) d.h = lds_tr(pa1 + m * DA + (j >> 1) * 512); else d.l = lds_tr(pa0 + m * DA + (j >> 1) * 512);
    } else if ((j & 1) == 0) {
      const u32x4 v = ldsa((j ? pa1 : pa0) + m * DA);
      d.l = u32x2{v[0], v[1]}, d.h = u32x2{v[2], v[3]};
    }
    __builtin_amdgcn_sched_barrier(0);
  }
  __device__ __forceinline__ void rb(int i) {
    b[pn(i)][pk(i)] = ldsa((pk(i) ? pb1 : pb0) + pn(i) * 2048);
    __builtin_amdgcn_sched_barrier(0);
  }
  __device__ __forceinline__ void mm(int i) {
    const AF& x = a[(i >> 3) & 1][pk(i)];
    const u32x4 av = u32x4{x.l[0], x.l[1], x.h[0], x.h[1]};
    asm volatile("v_mfma_f32_16x16x32_bf16 %0, %1, %2, %0" : "+v"(acc[i >> 3][pn(i)]) : "v"(av), "v"(b[pn(i)][pk(i)]));
    __builtin_amdgcn_sched_barrier(0);
    if (i < 4) ra(1, i);
    if (i >= 8 && i < 12) ra(2, i - 8);
    if (i >= 15 && i < 19) ra(3, i - 15);
  }
  static __device__ __forceinline__ void add(uint32_t& p, int adv) {
    p += adv;
    asm volatile("" : "+v"(p));
    __builtin_amdgcn_sched_barrier(0);
  }
  template <int BAR> __device__ __forceinline__ void step(int& k, int k1, uint32_t cnt) {
    int adv;
#pragma unroll
    for (int i = 0; i < 32; ++i) {
      mm(i);
      if (i == 12)
        asm volatile("s_cmp_eq_u32 %1, %2\n\ts_cselect_b32 %0, %3, %4" : "=s"(adv) : "s"(so), "n"((TR ? 2 : 3) * G0_STAGE), "s"((TR ? -2 : -3) * G0_STAGE), "s"(G0_STAGE) : "scc");
      if (i == 13) asm volatile("s_add_u32 %0, %0, %2\n\ts_add_u32 %1, %1, 1" : "+s"(so), "+s"(k) : "s"(adv) : "scc");
      if (i == 14) add(pb0, adv);
      if (i == 19) add(pa0, adv);
      if (i == 20) add(pa1, adv);
      if (i == 21) add(pb1, adv);
      if (i == 23) __builtin_amdgcn_s_waitcnt(0xC07F);
      if (i == 23 && BAR == 1) asm volatile("s_barrier" ::: "memory");
      if (i == 23 && BAR == 2) {
        wait_vm<0>();
        lds_signal<SW_S0>(4);
        __builtin_amdgcn_s_setprio(1);
        __builtin_amdgcn_sched_barrier(0);
      }
      if (i == 23 && !BAR) {
        uint32_t c, t;
        asm volatile(
            "s_sub_u32 %3, %4, %5\n\ts_lshl_b32 %3, %3, 2\n\tv_mov_b32 %1, %3\n\ts_add_u32 %3, %3, %6\n1:\n\tds_read_b32 %0, %1 offset:%7\n\t"
            "s_waitcnt lgkmcnt(0)\n\tv_readfirstlane_b32 %2, %0\n\ts_cmp_lt_u32 %2, %3\n\ts_cbranch_scc1 1b"
            : "=&v"(a[0][0].l[0]), "=&v"(a[0][0].l[1]), "=&s"(c), "=&s"(t)
            : "s"(k), "s"(k1), "s"(cnt), "n"(SW_LD * 4)
            : "scc", "memory");
        __builtin_amdgcn_sched_barrier(0);
      }
      __builtin_amdgcn_sched_barrier(0);
      if (i == 24) rb(i);
      if (i >= 24 && i < (TR ? 28 : 26)) ra(0, TR ? i - 24 : 2 * (i - 24));
      if (i > 24) rb(i);
    }
  }
  __device__ __forceinline__ void run(int k, int n, uint32_t cnt) {
    const int k1 = k + n - 3;
    asm volatile("" : "+v"(pa0), "+v"(pa1), "+v"(pb0), "+v"(pb1), "+s"(so));
    if (!TR) bar_lds();
    ra(0, 0), ra(0, 1), rb(0), ra(0, 2), ra(0, 3);
#pragma unroll
    for (int i = 1; i < 8; ++i) rb(i);
    if (TR) {
#pragma nounroll
      do step<2>(k, 0, 0); while (k == 0);
    }
#pragma nounroll
    do step<1>(k, 0, 0); while (k != k1);
#pragma nounroll
    do step<0>(k, k1 + 1, cnt); while (k != k1 + 2);
  }
};

__device__ __forceinline__ void mfma_role(const Args& A, char* smem, int wave, int lane) {
  char* const ws = A.ws;
  const Tile T = tile_map();
  const int tm = T.tm, tn = T.tn;
  lds_u32* const sw = (lds_u32*)(smem + L_SYNC);
  const uint32_t U = (uint32_t)(size_t)(sw + 1);
  const int wr = wave >> 1, wc = wave & 1;
  int fr = lane & 15, fq = lane >> 4;
  {
    if (T.late) bar_lds();
    if (wave == 3) wait_vm<0>();
    __builtin_amdgcn_s_setprio(1);
  }
  {
    f32x4 acc[4][4];
#pragma unroll
    for (int m = 0; m < 4; ++m)
#pragma unroll
      for (int n = 0; n < 4; ++n) acc[m][n] = Z4;
    const cf* prm = reinterpret_cast<const cf*>(smem + L_PRM) + wc * 32;
    cf* part = reinterpret_cast<cf*>(smem + L_PART) + wc * 32;
    cf lam[2], l16[2], l32[2], s[2];
    int ao;
    const __amdgpu_buffer_rsrc_t rag = __builtin_amdgcn_make_buffer_rsrc((void*)(ws + WS_AGG), 0, 512 << 10, 0x00020000);
    auto scan = [&](int i, int jp) {
      const int m = i >> 2;
      if ((i & 3) == 0 || i == 1) asm volatile("" : "+v"(acc[m][2 * jp]), "+v"(acc[m][2 * jp + 1]));
      if (i == 1) s[jp] = cf{acc[0][2 * jp][0], acc[0][2 * jp + 1][0]};
      s[jp] = cmad(s[jp], lam[jp], cf{acc[m][2 * jp][i & 3], acc[m][2 * jp + 1][i & 3]});
      __builtin_amdgcn_sched_barrier(0);
    };
    {
      Core<false> g{acc};
      g.pa0 = lds_base(smem) + (wr * 64 + (fr >> 2) * 16 + (fr & 3)) * 128 + (fq ^ (fr & 7)) * 16;
      g.pb0 = lds_base(smem) + (128 + wc * 64 + fr) * 128 + (fq ^ (fr & 7)) * 16;
      g.pa1 = g.pa0 ^ 64, g.pb1 = g.pb0 ^ 64;
#pragma unroll
      for (int m = 0; m < 4; ++m) asm volatile("" : "+v"(acc[m][0]), "+v"(acc[m][1]), "+v"(acc[m][2]), "+v"(acc[m][3]));
      g.run(0, 16, 16);
#pragma unroll
      for (int i = 0; i < 32; ++i) {
        g.mm(i);
        if (i == 1) {
          asm volatile("" : "+v"(lane));
          lam[0] = prm[lane & 15], lam[1] = prm[16 + (lane & 15)];
          __builtin_amdgcn_s_setprio(3);
          __builtin_amdgcn_sched_barrier(0);
        }
        if (i >= 8 && i < 14) scan(1 + (i - 8) % 3, (i - 8) / 3);
        if (i >= 14 && i < 22) scan(4 + ((i - 14) & 3), i < 18);
        if (i >= 22) scan(8 + ((i - 22) & 3) + (i >= 30 ? 4 : 0), i >= 26 && i < 30);
        if (i == 25) {
          lds_signal<SW_G0E>(U);
          asm volatile("" : "+v"(lane));
          fr = lane & 15;
          fq = lane >> 4;
#pragma unroll
          for (int jp = 0; jp < 2; ++jp) l16[jp] = part[64 + jp * 16 + fr], l32[jp] = part[128 + jp * 16 + fr];
          ao = ((2 * tm + wr) * 1024 + tn * 64 + wc * 32 + lane) * 16 + ((lane & 32) << 15);
          asm volatile("" : "+v"(ao));
          __builtin_amdgcn_sched_barrier(0);
        }
      }
      scan(14, 0), scan(15, 0);
#pragma unroll
      for (int i = 12; i < 16; ++i) scan(i, 1);
    }
    const uint64_t m1 = 0xFFFF0000FFFF0000ull, m2 = 0xFFFFFFFF00000000ull;
    cf pe[2], a01[2], wg[2];
#pragma unroll
    for (int jp = 0; jp < 2; ++jp) {
      const u32x2 ex = __builtin_amdgcn_permlane16_swap(f2u(s[jp].x), f2u(s[jp].x), false, false);
      const u32x2 ey = __builtin_amdgcn_permlane16_swap(f2u(s[jp].y), f2u(s[jp].y), false, false);
      pe[jp] = cf{u2f(ex[0]), u2f(ey[0])};
      const cf ap = cmad(pe[jp], l16[jp], cf{u2f(ex[1]), u2f(ey[1])});
      const u32x2 hx = __builtin_amdgcn_permlane32_swap(f2u(ap.x), f2u(ap.x), false, false);
      const u32x2 hy = __builtin_amdgcn_permlane32_swap(f2u(ap.y), f2u(ap.y), false, false);
      a01[jp] = cf{u2f(hx[0]), u2f(hy[0])};
      wg[jp] = cmad(a01[jp], l32[jp], cf{u2f(hx[1]), u2f(hy[1])});
    }
    {
      const cf ws_ = sel_c(m1, wg[1], wg[0]);
      __builtin_amdgcn_raw_buffer_store_b128(u32x4{f2u(ws_.x), 1u, f2u(ws_.y), 1u}, rag, ao, 0, 16);
      if (wr == 0) {
        part[lane & 31] = ws_;
        wait_lgkm0();
      }
      lds_signal<SW_PUB>(U);
      if (wr) lds_wait<SW_PUB, 0>(U, 4);
    }
    __builtin_amdgcn_s_setprio(2);
    cf v[16], pw;
#pragma unroll
    for (int jp = 1; jp >= 0; --jp) {
      const cf cof = prm[64 + jp * 16 + fr];
      const cf p1 = sel_c(m1, l16[jp], cf{1.f, 0.f});
      cf cc = sel_c(m1, pe[jp], cf{0.f, 0.f});
      cc = sel_c(m2, cmad(p1, a01[jp], cc), cc);
      cf p = sel_c(m2, cmul(p1, l32[jp]), p1);
      if (wr) {
        cc = cmad(p, part[jp * 16 + fr], cc);
        p = cmul(p, part[192 + jp * 16 + fr]);
      }
      pw = cmul(cmul(p, lam[jp]), cof);
#pragma unroll
      for (int i = 0; i < 16; ++i) {
        const cf u = cf{acc[i >> 2][2 * jp][i & 3], acc[i >> 2][2 * jp + 1][i & 3]};
        v[i] = cmul(i ? u : cmad(cc, lam[jp], u), cof);
        asm volatile("" : "+v"(v[i]));
      }
      asm volatile("" : "+v"(pw));
      if (jp) {
        lds_wait<SW_G0E, 0>(U, 4);
#pragma unroll
        for (int i = 0; i < 8; ++i)
          sta4(lds_base(smem) + (T.late ? 0 : 3 * G0_STAGE) + wave * 8192 + i * 1024 + lane * 16,
               u32x4{f2u(v[2 * i].x), f2u(v[2 * i].y), f2u(v[2 * i + 1].x), f2u(v[2 * i + 1].y)});
        *reinterpret_cast<cf*>(smem + L_TB + 2048 + wave * 512 + lane * 8) = pw;
        wait_lgkm0();
        lds_signal<SW_HO>(U);
      }
    }
    __builtin_amdgcn_s_setprio(3);
    states(ws, smem, wave, lane, 0, v, pw, lam[0]);
  }
  if (T.late) __builtin_amdgcn_s_setprio(0);
  publish8<0>(ws, 4 * tm + (T.late ? 2 : T.nt >> 2));
  if (T.late) {
    bar();
    bar();
  }
}

__device__ __forceinline__ void gemm1(const Args& A, char* smem, int wave, int lane) {
  char* const ws = A.ws;
  const Tile T = tile_map();
  const int tm = T.tm, nt = T.nt;
  const uint32_t U = 4;
  const int wr = wave >> 1, wc = wave & 1, fr = lane & 15, fq = lane >> 4;
  f32x4 acc[4][4];
  if (T.late) {
    const char* px = reinterpret_cast<const char*>(A.xB + tm * 128 * 1024 + nt * 128 + wc * 64);
    const uint32_t pxo = (wr * 64 + g1_rot(wr) * 16 + fq * 4) * 2048 + fr * 8;
    const u32x2 dw = reinterpret_cast<const u32x2*>(A.Dsk + nt * 128 + wc * 64)[fr];
    u32x2 xw[16];
#pragma unroll
    for (int i = 0; i < 16; ++i) xw[i] = *reinterpret_cast<const u32x2*>(px + ((pxo ^ ((i >> 2) << 15)) + (i & 3) * 2048));
#pragma unroll
    for (int i = 0; i < 32; ++i) {
      const uint32_t a = xw[i >> 1][i & 1], d = dw[i & 1];
      const uint32_t p = pk_bf(u2f(a << 16) * u2f(d << 16), u2f(a & 0xFFFF0000u) * u2f(d & 0xFFFF0000u));
      acc[i >> 3][(i & 1) * 2][(i >> 1) & 3] = u2f(p << 16);
      acc[i >> 3][(i & 1) * 2 + 1][(i >> 1) & 3] = u2f(p & 0xFFFF0000u);
      if (i == 15) {
#pragma unroll
        for (int m = 0; m < 2; ++m) asm volatile("" : "+v"(acc[m][0]), "+v"(acc[m][1]), "+v"(acc[m][2]), "+v"(acc[m][3]));
#pragma unroll
        for (int j = 8; j < 16; j += 4) asm volatile("" : "+v"(xw[j]), "+v"(xw[j + 1]), "+v"(xw[j + 2]), "+v"(xw[j + 3]));
        __builtin_amdgcn_sched_barrier(0);
        lds_wait<SW_G0E, 4>(U, 4);
        stage_c(A, wave, lane, 0);
        stage_c(A, wave, lane, 1);
        __builtin_amdgcn_sched_barrier(0);
      }
    }
#pragma unroll
    for (int m = 0; m < 4; ++m) asm volatile("" : "+v"(acc[m][0]), "+v"(acc[m][1]), "+v"(acc[m][2]), "+v"(acc[m][3]));
    wait_vm<0>();
    lds_signal<SW_CP>(U);
    stage_c(A, wave, lane, 2);
  } else {
#pragma unroll
    for (int i = 0; i < 16; ++i) acc[i >> 2][i & 3] = Z4;
  }
  {
    Core<true> g{acc};
    g.pa0 = lds_base(smem) + G0_STAGE + wr * 8192 + ((fr >> 1) & 1) * 1024 + (8 * fq + (fr >> 2) + (fr & 2) * 2) * 16 + (fr & 1) * 8;
    g.pa1 = g.pa0 ^ 64;
    g.pb0 = lds_base(smem) + G0_STAGE + 16384 + (wc * 64 + fr) * 128 + (fq ^ (fr & 7)) * 16;
    g.pb1 = g.pb0 ^ 64;
#pragma unroll
    for (int m = 0; m < 4; ++m) asm volatile("" : "+v"(acc[m][0]), "+v"(acc[m][1]), "+v"(acc[m][2]), "+v"(acc[m][3]));
    lds_wait<SW_CP, 0>(U, 4);
    lds_wait<SW_HS, 0>(U, 8);
    __builtin_amdgcn_s_setprio(0);
    g.run(0, T.late ? 32 - NX : NX, 32);
    const __amdgpu_buffer_rsrc_t rp = __builtin_amdgcn_make_buffer_rsrc((void*)(ws + WS_PS), 0, 8 << 20, 0x00020000);
    int po = bid_x() * 32768 + wave * 8192 + lane * 16;
    asm volatile("" : "+v"(po));
    auto part = [&](int m) {
      if (T.late) return;
      asm volatile("" : "+v"(acc[m][0]), "+v"(acc[m][1]), "+v"(acc[m][2]), "+v"(acc[m][3]));
#pragma unroll
      for (int i = 0; i < 2; ++i) {
        const f32x4 a = acc[m][2 * i], b = acc[m][2 * i + 1];
        __builtin_amdgcn_raw_buffer_store_b128(u32x4{pk_bf(a[0], b[0]), pk_bf(a[1], b[1]), pk_bf(a[2], b[2]), pk_bf(a[3], b[3])}, rp,
                                               po + (2 * m + i) * 1024, 0, 16);
      }
      __builtin_amdgcn_sched_barrier(0);
    };
#pragma unroll
    for (int i = 0; i < 32; ++i) {
      g.mm(i);
      if (i >= 12 && (i & 7) == 4) part((i >> 3) - 1);
    }
    asm volatile("s_nop 7" : "+v"(acc[3][0]), "+v"(acc[3][1]), "+v"(acc[3][2]), "+v"(acc[3][3]));
    part(3);
  }
  asm volatile("" : "+v"(lane));
  if (!T.late) {
    lds_signal<SW_PL>(U);
    publish(ws, 64 + bid_x(), lane);
    warm_dev(smem, ws + WS_PS, bid_x() * 32768 + wave * 8192 + lane * 128);
  } else {
    const __amdgpu_buffer_rsrc_t ro = __builtin_amdgcn_make_buffer_rsrc((void*)A.out, 0, 16 << 20, 0x00020000);
    const int o = ((tm * 128 + wr * 64 + g1_rot(wr) * 16 + (lane >> 4) * 4) * 2048 + 1024 + nt * 128 + wc * 64 + (lane & 15) * 4) * 4;
    const uint32_t pp = lds_base(smem) + wave * 8192 + lane * 16;
    lds_wait<SW_PL, 0>(U, 4);
    u32x4 p[8];
#pragma unroll
    for (int i = 0; i < 8; ++i) p[i] = ldsa(pp + i * 1024);
#pragma unroll
    for (int m = 0; m < 4; ++m) {
      u32x4 y[4];
#pragma unroll
      for (int i = 0; i < 16; ++i) {
        const int n = i & 3, r = i >> 2;
        const uint32_t w = p[2 * m + (n >> 1)][r];
        y[r][n] = f2u(acc[m][n][r] + u2f((n & 1) ? w & 0xFFFF0000u : w << 16));
      }
      asm volatile("" : "+v"(y[0]), "+v"(y[1]), "+v"(y[2]), "+v"(y[3]));
#pragma unroll
      for (int r = 0; r < 4; ++r) __builtin_amdgcn_raw_buffer_store_b128(y[r], ro, (o ^ (m << 17)) + r * 8192, 0, 16);
      __builtin_amdgcn_sched_barrier(0);
    }
  }
}

}

extern "C" __global__ __launch_bounds__(1024, 4) void k_all(const uint32_t* x2, const unsigned short* w_r, const unsigned short* w_i,
                                                           const unsigned short* xB, const unsigned short* Lre,
                                                           const unsigned short* Lim, const unsigned short* Bre,
                                                           const unsigned short* Bim, const unsigned short* Cre,
                                                           const unsigned short* Cim, const unsigned short* ldt,
                                                           const unsigned short* Dsk, float* out, char* ws) {
  __shared__ __attribute__((aligned(256))) char smem_[LDS_BYTES];
  char* const smem = smem_ + 256;
  const Args A{x2, w_r, w_i, xB, Lre, Lim, Bre, Bim, Cre, Cim, ldt, Dsk, out, ws};
  const int tid = tid_x(), lane = tid & 63;
  int wave = __builtin_amdgcn_readfirstlane(tid >> 6);
  const uint32_t ldsb = lds_base(smem);
  {
    const int o = lane * 128 + (lane >> 5) * ((bid_x() << 9) & 0x7000);
    asm volatile(
        "s_cmp_lg_u32 %1, %3\n\ts_cbranch_scc1 1f\n\ts_getpc_b64 s[90:91]\n\ts_add_u32 s90, s90, k_all@rel32@lo+4\n\t"
        "s_addc_u32 s91, s91, k_all@rel32@hi+12\n\ts_mov_b32 m0, %2\n\ts_nop 0\n\tglobal_load_lds_dword %0, s[90:91]\n1:"
        ::"v"(o < 34624 - 16 ? o : 34624 - 16), "s"(wave), "s"(ldsb + L_SCR2), "n"(0)
        : "s90", "s91", "scc", "memory");
  }
  if (wave < 8) {
    const uint32_t o = (2 * wave + 16 * (lane >> 3)) * 16384 + (bid_x() & 7) * 2048 + ((bid_x() >> 3) & 15) * 128 + ((lane & 7) ^ ((lane >> 5) * 4)) * 16;
    if (bid_x() >= 128)
      asm volatile(
          "s_mov_b32 m0, %4\n\ts_nop 0\n\tglobal_load_lds_dwordx4 %0, %2 nt\n\tglobal_load_lds_dwordx4 %1, %2 offset:1024 nt\n\t"
          "global_load_lds_dwordx4 %0, %3 offset:2048 nt\n\tglobal_load_lds_dwordx4 %1, %3 offset:3072 nt"
          ::"v"(o), "v"(o + (2 << 20) - 1024), "s"(x2), "s"(x2 + (16384 - 2048) / 4), "s"(ldsb + 3 * G0_STAGE + wave * 4096)
          : "memory");
    else
      stage0(A, smem, wave & 3, lane, 0, 1 + (wave >> 2));
  } else if (wave < 12) {
    const int wc = (bid_x() >> 3) & 15;
    if (bid_x() >= 128 && (wc & 7) != 3 && wave != 10)
      warm_r(A, smem, lane, wave == 11 ? (wc & 8) | 3 : wc, wave == 11 ? (wc & 7) - ((wc & 7) > 3) : wave - 8);
  } else if (wave == 15) {
    const uint32_t o = tile_map().tn * 128 + (lane & 31) * 4;
    asm volatile(
        "s_mov_b32 m0, %4\n\ts_mov_b32 exec_hi, 0\n\tglobal_load_lds_dword %0, %1\n\ts_add_u32 m0, m0, 128\n\ts_nop 0\n\t"
        "global_load_lds_dword %0, %2\n\ts_add_u32 m0, m0, 128\n\ts_nop 0\n\tglobal_load_lds_dword %0, %3\n\ts_mov_b32 exec_hi, -1"
        ::"v"(o), "s"(Lre), "s"(Lim), "s"(ldt), "s"(ldsb + L_PRM + 512)
        : "memory", "scc");
  } else if (wave == 13) {
    warm1(smem, Dsk, (lane & 15) * 128);
  }
  asm volatile("" : "+s"(wave));
  bool g1 = false;
  if (__builtin_expect(wave >= 12, 0)) {
    if (wave == 12) *(volatile lds_u32*)((lds_u32*)(smem + L_SYNC) + lane) = 0;
    mfma_role(A, smem, wave - 12, lane);
    g1 = bid_x() < 128;
  } else if (__builtin_expect(wave >= 8, 0)) {
    loader_role(A, smem, wave - 8, lane);
    __builtin_amdgcn_s_waitcnt(0x0F70);
  } else {
    g1 = fft_role(A, smem, wave, lane);
    __builtin_amdgcn_s_waitcnt(0x0F70);
  }
  if (g1) gemm1(A, smem, wave & 3, (int)__builtin_amdgcn_mbcnt_hi(~0u, __builtin_amdgcn_mbcnt_lo(~0u, 0u)));
}
#if defined(__HIP_DEVICE_COMPILE__) && __HIP_DEVICE_COMPILE__
#pragma clang attribute pop
#endif

extern "C" void kernel_launch(void* const* d_in, const int* in_sizes, int n_in, void* d_out, int out_size, void* d_ws,
                              size_t ws_size, hipStream_t stream) {
  typedef const unsigned short* P;
  k_all<<<256, 1024, 0, stream>>>((const uint32_t*)d_in[0], (P)d_in[1], (P)d_in[2], (P)d_in[3], (P)d_in[4], (P)d_in[5], (P)d_in[6],
                                  (P)d_in[7], (P)d_in[8], (P)d_in[9], (P)d_in[10], (P)d_in[11], (float*)d_out, (char*)d_ws);
}
```

```cpp
#include <hip/hip_runtime.h>
#include <stdint.h>

namespace {

#if defined(__HIP_DEVICE_COMPILE__) && __HIP_DEVICE_COMPILE__
#pragma clang attribute push(__attribute__((target("no-packed-fp32-ops"))), apply_to = function)
#endif
__device__ __forceinline__ int tid_x() { return __builtin_amdgcn_workitem_id_x(); }
__device__ __forceinline__ int bid_x() { return __builtin_amdgcn_workgroup_id_x(); }

typedef float cf __attribute__((ext_vector_type(2)));
typedef short bf16x8 __attribute__((ext_vector_type(8)));
typedef short s16x4 __attribute__((ext_vector_type(4)));
typedef __bf16 b16x2 __attribute__((ext_vector_type(2)));
typedef float f32x4 __attribute__((ext_vector_type(4)));
typedef uint32_t u32x4 __attribute__((ext_vector_type(4)));
typedef uint32_t u32x2 __attribute__((ext_vector_type(2)));
typedef unsigned short u16;
typedef unsigned long long u64;
typedef __attribute__((address_space(3))) uint32_t lds_u32;

__device__ __forceinline__ float u2f(uint32_t u) { return __builtin_bit_cast(float, u); }
__device__ __forceinline__ uint32_t f2u(float f) { return __builtin_bit_cast(uint32_t, f); }
__device__ __forceinline__ float bf2f(u16 b) { return u2f((uint32_t)b << 16); }
__device__ __forceinline__ float rbf(float f) { return bf2f(__builtin_bit_cast(u16, (__bf16)f)); }
__device__ __forceinline__ uint32_t pk_bf(float a, float b) {
  const b16x2 h = b16x2{(__bf16)a, (__bf16)b};
  return __builtin_bit_cast(uint32_t, h);
}
__device__ __forceinline__ cf fin(cf v) {
  float x = v.x, y = v.y;
  asm("" : "+v"(x), "+v"(y));
  return cf{x, y};
}
__device__ __forceinline__ cf cmul(cf a, cf b) {
  return cf{__builtin_fmaf(a.x, b.x, -(a.y * b.y)), __builtin_fmaf(a.x, b.y, a.y * b.x)};
}
__device__ __forceinline__ cf cmad(cf a, cf b, cf c) {
  return cf{__builtin_fmaf(a.x, b.x, __builtin_fmaf(-a.y, b.y, c.x)), __builtin_fmaf(a.x, b.y, __builtin_fmaf(a.y, b.x, c.y))};
}
__device__ __forceinline__ cf cisr(int k, float inv_n) {
  const float r = (float)k * inv_n;
  return cf{__builtin_amdgcn_cosf(r), -__builtin_amdgcn_sinf(r)};
}
template <int N> __device__ __forceinline__ cf cis(int k) { return cisr(k, 1.0f / N); }
template <int N> __device__ __forceinline__ void wait_vm() { asm volatile("s_waitcnt vmcnt(%0)" ::"n"(N) : "memory"); }
__device__ __forceinline__ void wait_lgkm0() { asm volatile("s_waitcnt lgkmcnt(0)" ::: "memory"); }
__device__ __forceinline__ void glds2_dev(const void* sb, uint32_t v0, uint32_t lds_dst) {
  uint32_t keep;
  asm volatile(
      "s_mov_b32 %0, m0\n\ts_mov_b32 m0, %3\n\ts_nop 0\n\tglobal_load_lds_dwordx4 %1, %2 sc1\n\t"
      "global_load_lds_dwordx4 %1, %2 offset:1024 sc1\n\ts_mov_b32 m0, %0"
      : "=&s"(keep)
      : "v"(v0), "s"(sb), "s"(lds_dst)
      : "memory");
}
__device__ __forceinline__ void gdev2(const void* sb, uint32_t v0, uint32_t v1, uint32_t lds_dst) {
  uint32_t keep;
  asm volatile(
      "s_mov_b32 %0, m0\n\ts_mov_b32 m0, %4\n\ts_nop 0\n\tglobal_load_lds_dwordx4 %1, %3 sc1\n\t"
      "global_load_lds_dwordx4 %2, %3 offset:1024 sc1\n\ts_mov_b32 m0, %0"
      : "=&s"(keep)
      : "v"(v0), "v"(v1), "s"(sb), "s"(lds_dst)
      : "memory");
}
__device__ __forceinline__ void glds4(const void* sb0, const void* sb1, uint32_t v0, uint32_t v1, uint32_t v2, uint32_t v3,
                                      uint32_t lds_dst) {
  uint32_t keep;
  asm volatile(
      "s_mov_b32 %0, m0\n\ts_mov_b32 m0, %7\n\ts_nop 0\n\tglobal_load_lds_dwordx4 %1, %5\n\t"
      "global_load_lds_dwordx4 %2, %5 offset:1024\n\tglobal_load_lds_dwordx4 %3, %6 offset:2048\n\t"
      "global_load_lds_dwordx4 %4, %6 offset:3072\n\ts_mov_b32 m0, %0"
      : "=&s"(keep)
      : "v"(v0), "v"(v1), "v"(v2), "v"(v3), "s"(sb0), "s"(sb1), "s"(lds_dst)
      : "memory");
}
__device__ __forceinline__ constexpr uint32_t lds_base(char*) { return 256; }
__device__ __forceinline__ void bar() {
  __builtin_amdgcn_sched_barrier(0);
  asm volatile("" ::: "memory");
  __builtin_amdgcn_s_barrier();
  asm volatile("" ::: "memory");
  __builtin_amdgcn_sched_barrier(0);
}
__device__ __forceinline__ void bar_lds() {
  asm volatile("s_waitcnt lgkmcnt(0)" ::: "memory");
  bar();
}
template <int W> __device__ __forceinline__ void lds_signal(uint32_t U) {
  asm volatile("s_mov_b64 exec, 1\n\tds_add_u32 %0, %0 offset:%1\n\ts_mov_b64 exec, -1" ::"v"(U), "n"(W * 4 - 4) : "memory");
}
template <int W> __device__ __forceinline__ uint32_t lds_peek(uint32_t U) {
  return __builtin_amdgcn_readfirstlane(*(volatile lds_u32*)(size_t)(U + (W * 4 - 4)));
}
template <int W, int SLEEP> __device__ __forceinline__ void lds_wait(uint32_t U, uint32_t n) {
  uint32_t v, c;
  if (SLEEP)
    asm volatile(
        "s_branch 2f\n1:\n\ts_sleep %5\n2:\n\tds_read_b32 %0, %2 offset:%4\n\ts_waitcnt lgkmcnt(0)\n\tv_readfirstlane_b32 %1, %0\n\t"
        "s_cmp_lt_u32 %1, %3\n\ts_cbranch_scc1 1b"
        : "=&v"(v), "=&s"(c)
        : "v"(U), "s"(n * U), "n"(W * 4 - 4), "n"(SLEEP)
        : "memory", "scc");
  else
    asm volatile(
        "1:\n\tds_read_b32 %0, %2 offset:%4\n\ts_waitcnt lgkmcnt(0)\n\tv_readfirstlane_b32 %1, %0\n\t"
        "s_cmp_lt_u32 %1, %3\n\ts_cbranch_scc1 1b"
        : "=&v"(v), "=&s"(c)
        : "v"(U), "s"(n * U), "n"(W * 4 - 4)
        : "memory", "scc");
}
__device__ __forceinline__ float sel_f(uint64_t m, float a, float b) {
  float r;
  asm("v_cndmask_b32_e64 %0, %1, %2, %3" : "=v"(r) : "v"(b), "v"(a), "s"(m));
  return r;
}
__device__ __forceinline__ cf sel_c(uint64_t m, cf a, cf b) { return cf{sel_f(m, a.x, b.x), sel_f(m, a.y, b.y)}; }
__device__ __forceinline__ u64 ld_dev(const u64* p) { return __hip_atomic_load(p, __ATOMIC_RELAXED, __HIP_MEMORY_SCOPE_AGENT); }
__device__ __forceinline__ void st_dev(u64* p, u64 v) { __hip_atomic_store(p, v, __ATOMIC_RELAXED, __HIP_MEMORY_SCOPE_AGENT); }
__device__ __forceinline__ void st_dev(uint32_t* p, uint32_t v) {
  __hip_atomic_store(p, v, __ATOMIC_RELAXED, __HIP_MEMORY_SCOPE_AGENT);
}
__device__ __forceinline__ f32x4 mfma_b(u32x4 a, u32x4 b, f32x4 c) {
  return __builtin_amdgcn_mfma_f32_16x16x32_bf16(__builtin_bit_cast(bf16x8, a), __builtin_bit_cast(bf16x8, b), c, 0, 0, 0);
}
struct PQ {
  u32x4 p, q;
};
template <int V> struct IC {
  static constexpr int value = V;
};
constexpr f32x4 Z4 = f32x4{0.f, 0.f, 0.f, 0.f};
constexpr float S16 = 0.283235f;
constexpr unsigned SPIN_MAX = 1u << 20;

constexpr size_t WS_EPOCH = 0;
constexpr size_t WS_F1 = 4096;
constexpr size_t WS_P2 = 8192;
constexpr size_t WS_HF = 1u << 19;
constexpr size_t WS_AGG = 1u << 20;
constexpr size_t WS_ZB = 2u << 20;
constexpr size_t WS_ZC = 8u << 20;
constexpr size_t WS_HS = 18u << 20;
constexpr size_t WS_PS = 26u << 20;

constexpr int NX = 14;
constexpr int G0_STAGE = 32768;
constexpr int L_Z = 0;
constexpr int L_X = 131072;
constexpr int L_TB = L_X + 16384;
constexpr int L_WS = L_TB + 4096;
constexpr int L_PART = L_WS + 4096;
constexpr int L_GRP = L_PART + 3584;
constexpr int L_SYNC = -256;
constexpr int L_PRM = L_GRP + 3072;
constexpr int L_SCR2 = L_PRM + 1024;
constexpr int LDS_BYTES = L_SCR2 + 256 + 256;
enum { SW_GRP = 2, SW_PUB, SW_CP, SW_S0, SW_HD, SW_G1, SW_FS, SW_G0E, SW_HS, SW_LD, SW_LD2, SW_F1, SW_PX, SW_FR, SW_PL, SW_P2, SW_HO };

struct Args {
  const uint32_t* x2;
  const u16 *w_r, *w_i, *xB, *Lre, *Lim, *Bre, *Bim, *Cre, *Cim, *ldt, *Dsk;
  float* out;
  char* ws;
};

struct Tile {
  int tm, tn, nt, late;
};
__device__ __forceinline__ Tile tile_map() {
  const int bid = bid_x(), late = bid >> 7, cq = (bid & 1) * 4 + ((bid >> 5) & 3);
  return Tile{(bid >> 1) & 15, late * 8 + cq, cq, late};
}
__device__ __forceinline__ u32x4 lds4(const void* p) { return *reinterpret_cast<const u32x4*>(p); }
__device__ __forceinline__ u32x4 ldsa(uint32_t a) { return *(__attribute__((address_space(3))) u32x4*)(size_t)a; }
__device__ __forceinline__ void lds_cf2(const void* p, cf& a, cf& b) {
  const f32x4 v = *reinterpret_cast<const f32x4*>(p);
  a = cf{v[0], v[1]};
  b = cf{v[2], v[3]};
}
template <int W> __device__ __forceinline__ void group_sync(uint32_t U, uint32_t n) {
  wait_lgkm0();
  lds_signal<W>(U);
  lds_wait<W, 0>(U, n);
}
__device__ __forceinline__ int k_rot() { return (bid_x() & 6) * 2 + (bid_x() & 1) * 8; }
__device__ __forceinline__ void stage0(const Args& A, char* smem, int wave, int lane, int kt, int parts = 3) {
  const Tile T = tile_map();
  const int prow = lane >> 3;
  const uint32_t va = (T.tm * 128 + wave * 32 + prow) * 2048 + ((lane & 7) ^ (prow & 3)) * 16;
  const uint32_t vb = (T.tn * 64 + wave * 16 + prow) * 2048 + ((lane & 7) ^ prow) * 16;
  const uint32_t dst = lds_base(smem) + wave * 4096 + (kt & 3) * G0_STAGE;
  kt = (kt + k_rot()) & 15;
  if (parts & 1) glds4(A.xB + kt * 64, A.xB + kt * 64, va, va + 15360, (va ^ 64) + 2 * 15360, (va ^ 64) + 3 * 15360, dst);
  if (parts & 2) glds4(A.Bre + kt * 64, A.Bim + kt * 64 - 1024, vb, vb + 15360, vb, vb + 15360, dst + 16384);
}
__device__ __forceinline__ int g1_kt(int late, int s) {
  const int c = 2 * tile_map().nt, g = c & 8;
  if (s < 2) return late * 16 + c + s;
  if (s < (late ? 10 : 8)) {
    int j = s - 2;
    if (!late && j >= ((c + 2) & 7)) j += 2;
    return ((j + 6) & 7) + (late ? g ^ 8 : g);
  }
  const int j = late ? s - 10 : s + 14 - NX;
  return 16 + (j < c ? j : j + 2);
}
__device__ __forceinline__ int g1_rot(int wr) { return (tile_map().nt ^ (wr << 1)) & 3; }
__device__ __forceinline__ uint32_t g1_vc(int wave, int lane) {
  return (tile_map().nt * 128 + (wave >> 1) * 64 + (wave & 1) * 2 + (lane >> 3) * 4) * 2048 + ((lane & 7) ^ (lane >> 3)) * 16;
}
__device__ __forceinline__ void stage_c(const Args& A, int wave, int lane, int s) {
  const int kt = g1_kt(tile_map().late, s);
  const u16* src = ((kt & 1) ? A.Cim : A.Cre) + (kt >> 1) * 64;
  const uint32_t vc = g1_vc(wave, lane);
  glds4(src, src, vc, vc + 65536 - 1024, vc, vc + 65536 - 1024, 256 + (s + 1) * G0_STAGE + 16384 + wave * 4096);
}
__device__ __forceinline__ void warm1(char* smem, const void* base, uint32_t off) {
  uint32_t keep;
  asm volatile("s_mov_b32 %0, m0\n\ts_mov_b32 m0, %3\n\ts_nop 0\n\tglobal_load_lds_dword %1, %2\n\ts_mov_b32 m0, %0"
               : "=&s"(keep)
               : "v"(off), "s"(base), "s"(lds_base(smem) + L_SCR2)
               : "memory");
}
__device__ __forceinline__ void warm_dev(char* smem, const void* base, uint32_t off) {
  uint32_t keep;
  asm volatile("s_mov_b32 %0, m0\n\ts_mov_b32 m0, %3\n\ts_nop 0\n\tglobal_load_lds_dword %1, %2 sc1\n\ts_mov_b32 m0, %0"
               : "=&s"(keep)
               : "v"(off), "s"(base), "s"(lds_base(smem) + L_SCR2)
               : "memory");
}
__device__ __forceinline__ void warm_r(const Args& A, char* smem, int lane, int c, int i) {
  const int bid = bid_x(), tm = ((bid >> 1) & 3) + (c & 3) * 4, nt = (bid & 1) * 4 + (c >> 2);
  uint64_t px = (uint64_t)A.xB, pbr = (uint64_t)A.Bre, pbi = (uint64_t)A.Bim;
  asm volatile("" : "+s"(px), "+s"(pbr), "+s"(pbi));
  px += (tm * 128 + (nt & 3) * 32 + (lane & 31)) * 2048;
  const uint64_t pb = ((lane & 16) ? pbi : pbr) + (nt * 64 + (c & 3) * 16 + (lane & 15)) * 2048;
  const int hi = lane >> 5;
  const bool k2 = i == 4 || i == 6;
  const int sl = i < 3 ? i + 1 : (k2 ? i - 4 : i + 1) + hi;
  const uint64_t pfp = ((k2 || (i < 3 && hi)) ? pb + (k2 ? 1 << 20 : 0) : px) + ((sl + k_rot()) & 15) * 128;
  uint32_t keep;
  asm volatile("s_mov_b32 %0, m0\n\ts_mov_b32 m0, %2\n\ts_nop 0\n\tglobal_load_lds_dword %1, off\n\ts_mov_b32 m0, %0"
               : "=&s"(keep)
               : "v"(pfp), "s"(lds_base(smem) + L_SCR2)
               : "memory");
}
__device__ __forceinline__ u32x2 lds_tr(uint32_t a) {
  return __builtin_bit_cast(u32x2, __builtin_amdgcn_ds_read_tr16_b64_v4i16((__attribute__((address_space(3))) s16x4*)(size_t)a));
}
__device__ __forceinline__ void sta4(uint32_t a, u32x4 v) { *(__attribute__((address_space(3))) u32x4*)(size_t)a = v; }
template <int P> __device__ __forceinline__ void prio(bool on) {
  __builtin_amdgcn_sched_barrier(0);
  if (on) __builtin_amdgcn_s_setprio(P);
  __builtin_amdgcn_sched_barrier(0);
}
__device__ __forceinline__ void row_head(u32x4 (&x)[2][2], const PQ& m16, const PQ& m1, const cf (&tw)[2][4], bool on) {
  f32x4 dr[2][2], di[2][2];
#pragma unroll
  for (int k = 0; k < 2; ++k) {
    const f32x4 ur = mfma_b(x[k][0], m16.p, Z4), ui = mfma_b(x[k][0], m16.q, Z4);
    const u32x4 xn = x[k][1] ^ 0x80008000u;
    dr[k][0] = mfma_b(x[k][1], m1.p, ur), di[k][0] = mfma_b(x[k][1], m1.q, ui);
    dr[k][1] = mfma_b(xn, m1.p, ur), di[k][1] = mfma_b(xn, m1.q, ui);
  }
  prio<0>(on);
#pragma unroll
  for (int i = 0; i < 16; ++i) {
    const int k = i >> 3, t = (i >> 2) & 1, r = i & 3;
    const cf v = fin(cmul(cf{dr[k][t][r], di[k][t][r]}, tw[t][r]));
    dr[k][t][r] = v.x, di[k][t][r] = v.y;
  }
  prio<1>(on);
#pragma unroll
  for (int i = 0; i < 16; ++i) x[i >> 3][(i >> 2) & 1][i & 3] = pk_bf(dr[i >> 3][(i >> 2) & 1][i & 3], di[i >> 3][(i >> 2) & 1][i & 3]);
}
template <int W, bool B = false, bool D = false>
__device__ __forceinline__ void poll_count(char* ws, int idx, uint32_t n, uint32_t U, const void* src = nullptr, uint32_t v0 = 0, uint32_t v1 = 0,
                                           uint32_t dst = 0) {
  uint32_t c0, c1, c2, t, lim = SPIN_MAX;
#define ASK(c) "global_atomic_add " c ", %5, %5, %6 sc0\n\t"
#define CHK(c) "s_waitcnt vmcnt(2)\n\tv_readfirstlane_b32 %3, " c "\n\ts_cmp_ge_u32 %3, %7\n\ts_cbranch_scc1 2f\n\t" ASK(c)
  asm volatile(
      "s_mov_b64 exec, 1\n\t" ASK("%0") "s_sleep 4\n\t" ASK("%1") "s_sleep 4\n\t" ASK("%2") ".if %10\n\ts_barrier\n.endif\n1:\n\t" CHK("%0") CHK("%1") CHK("%2")
      "s_sub_u32 %4, %4, 1\n\ts_cbranch_scc0 1b\n2:\n\tds_add_u32 %8, %8 offset:%9\n\ts_mov_b64 exec, -1\n.if %11\n\t"
      "s_mov_b32 %3, m0\n\ts_mov_b32 m0, %15\n\ts_nop 0\n\tglobal_load_lds_dwordx4 %13, %12 sc1\n\tglobal_load_lds_dwordx4 %13, %12 offset:1024 sc1\n\t"
      "global_load_lds_dwordx4 %14, %12 offset:2048 sc1\n\tglobal_load_lds_dwordx4 %14, %12 offset:3072 sc1\n\ts_mov_b32 m0, %3\n\t"
      "s_waitcnt vmcnt(4)\n.else\n\ts_waitcnt vmcnt(0)\n.endif"
      : "=&v"(c0), "=&v"(c1), "=&v"(c2), "=&s"(t), "+&s"(lim)
      : "v"(0), "s"(ws + WS_HF + idx * 128), "s"(n), "v"(U), "n"(W * 4 - 4), "n"(B), "n"(D), "s"(D ? src : ws), "v"(v0), "v"(v1), "s"(dst)
      : "memory", "scc");
#undef ASK
#undef CHK
}
__device__ __forceinline__ bool fft_role(const Args& A, char* smem, int f, int lane) {
  char* const ws = A.ws;
  const Tile T = tile_map();
  const int bid = bid_x(), li = bid & 127;
  int l15 = lane & 15, q = lane >> 4;
  lds_u32* const sw = (lds_u32*)(smem + L_SYNC);
  const uint32_t U = (uint32_t)(size_t)(sw + 1), ldsb = lds_base(smem);
  const __amdgpu_buffer_rsrc_t rzb = __builtin_amdgcn_make_buffer_rsrc((void*)(ws + WS_ZB), 0, 16 << 20, 0x00020000);
  auto fsync = [&](int n) { group_sync<SW_FS>(U, 8 * n); };
  auto bars = [&](int n) {
#pragma nounroll
    for (int i = 0; i < n; ++i) bar();
  };
  u64 epoch = 0;
  if (f == 0) epoch = ld_dev((const u64*)(ws + WS_EPOCH));
  if (f == 7) {
    const __amdgpu_buffer_rsrc_t ra = __builtin_amdgcn_make_buffer_rsrc((void*)(ws + WS_AGG), 0, 512 << 10, 0x00020000);
    __builtin_amdgcn_raw_buffer_store_b128(u32x4{0u, 0u, 0u, 0u}, ra, (T.tm * 2048 + T.tn * 64 + lane) * 16, 0, 16);
    __builtin_amdgcn_raw_buffer_store_b128(u32x4{0u, 0u, 0u, 0u}, ra, (T.tm * 2048 + 1024 + T.tn * 64 + lane) * 16, 0, 16);
    if (!T.late && lane < (T.tn ? 1 : 4)) st_dev((uint32_t*)(ws + WS_HF + (lane ? 4 * T.tm + lane - 1 : 64 + li) * 128), 0u);
    if (lane == 0) st_dev((uint32_t*)(ws + WS_P2) + bid, 0u);
  }
  __builtin_amdgcn_sched_barrier(0);
  auto ks_of = [&](int w) { return bid ? bid + 256 * w : (w < 4 ? 256 * w : 256 * w - 896); };
  {
    const int e = f & 3, n = 4 * q + e;
    uint32_t* tb = reinterpret_cast<uint32_t*>(smem + L_TB) + lane * 4;
    uint32_t* tx = reinterpret_cast<uint32_t*>(smem + L_X) + lane * 4;
    if (f < 4) {
      const cf c = cis<16>(n * l15) * S16;
      tb[e] = pk_bf(c.x, -c.y);
      tb[256 + e] = pk_bf(c.y, c.x);
      if (!T.late) {
        const cf d = cis<32>((2 * n + 1) * l15) * S16;
        tx[e] = pk_bf(d.x, -d.y);
        tx[256 + e] = pk_bf(d.y, d.x);
      }
    }
    if (!T.late) {
      *reinterpret_cast<cf*>(tx + 512 + (f >> 1) * 256 + (f & 1) * 2) = cis<512>(n * (16 * (f >> 2) + l15)) * (1.0f / (64 * S16 * S16));
      const cf w = cis<1024>(l15 + 16 * (f >> 2) + 32 * n);
      *reinterpret_cast<cf*>(tx + 1536 + (f >> 1) * 256 + (f & 1) * 2) = cf{w.y, -w.x};
      const int slot = f < 4 ? l15 : n, u = f < 4 ? q >> 1 : l15 & 1, j = f < 4 ? n & 7 : l15 >> 1;
      const bool own = (bid | (slot & 7)) == 0;
      const cf g = cis<2048>(j * (own ? slot * 128 : ks_of(slot & 7)));
      const int ua = bid ? 0 : (slot >> 2) & 1, ub = bid ? 1 : ua;
      const float al = u == ua ? ((own && f < 4) ? 1.f : 0.5f) : 0.f, be = (u == ub && !own) ? (slot < 8 ? 0.5f : -0.5f) : 0.f;
      const float sa = al + be, da = al - be;
      tx[2560 + (f >> 2) * 512 + e] = pk_bf(sa * g.x, -(f < 4 ? da : sa) * g.y);
      tx[2816 + (f >> 2) * 512 + e] = pk_bf((f < 4 ? sa : da) * g.y, da * g.x);
    }
  }
  if (!T.late) {
    if (f == 0) wait_vm<1>(); else if (f == 7) wait_vm<4>(); else wait_vm<0>();
  }
  bar_lds();
  PQ m16, m1, m8;
  cf tw[2][4];
  u32x4 wv[4];
  m16.p = lds4(smem + L_TB + lane * 16), m16.q = lds4(smem + L_TB + 1024 + lane * 16);
  PQ mk[2];
  auto col_mats = [&]() {
#pragma unroll
    for (int i = 0; i < 8; ++i) {
      const cf c = cis<256>((4 * q + (i & 3)) * (16 * l15 + 2 * f + (i >> 2))) * (1.0f / (16 * S16));
      uint32_t p = pk_bf(c.x, -c.y), o = pk_bf(c.y, c.x);
      asm volatile("" : "+v"(p), "+v"(o));
      mk[i >> 2].p[i & 3] = p, mk[i >> 2].q[i & 3] = o;
    }
    asm volatile("" : "+v"(mk[0].p), "+v"(mk[0].q), "+v"(mk[1].p), "+v"(mk[1].q));
  };
  auto col256 = [&](uint32_t img, int n, bool cj) {
    typedef __attribute__((address_space(3))) u32x2 lds_u32x2;
    const uint32_t own = img + f * 4096 + q * 512, xi = own + (q & 1) * 64 + l15 * 4;
    u32x4 x[4], d[4];
#pragma unroll
    for (int i = 0; i < 16; ++i) x[i >> 2][i & 3] = *(lds_u32*)(size_t)((xi ^ ((i & 4) * 16)) + (i >> 3) * 2048 + (i & 3) * 128);
    wait_lgkm0();
    lds_signal<SW_FS>(U);
#pragma unroll
    for (int i = 0; i < 4; ++i) {
      const f32x4 re = mfma_b(m16.p, x[i], Z4), im = mfma_b(m16.q, x[i], Z4);
      d[i] = u32x4{pk_bf(re[0], im[0]), pk_bf(re[1], im[1]), pk_bf(re[2], im[2]), pk_bf(re[3], im[3])};
    }
    const uint32_t xo = img + q * 8192 + (f >> 1) * 512 + l15 * 16 + (f & 1) * 8;
    asm volatile("" : "+v"(d[0]), "+v"(d[1]), "+v"(d[2]), "+v"(d[3]));
    lds_wait<SW_FS, 0>(U, 8 * n);
#pragma unroll
    for (int i = 0; i < 8; ++i) *(lds_u32x2*)(size_t)(xo + (i & 3) * 2048 + (i >> 2) * 256) = u32x2{d[i >> 2][i & 3], d[2 + (i >> 2)][i & 3]};
    fsync(n + 1);
#pragma unroll
    for (int i = 0; i < 4; ++i) x[i] = ldsa(own + l15 * 16 + (i >> 1) * 2048 + (i & 1) * 256);
    const uint32_t xt = img + f * 4096 + l15 * 128 + (q ^ (l15 & 7)) * 16;
#pragma unroll
    for (int i = 0; i < 4; ++i) {
      const f32x4 re = mfma_b(x[i], mk[i >> 1].p, Z4);
      f32x4 im = mfma_b(x[i], mk[i >> 1].q, Z4);
      if (cj) im = -im;
      d[i] = u32x4{pk_bf(re[0], im[0]), pk_bf(re[1], im[1]), pk_bf(re[2], im[2]), pk_bf(re[3], im[3])};
    }
    asm volatile("" : "+v"(d[0]), "+v"(d[1]), "+v"(d[2]), "+v"(d[3])::"memory");
#pragma unroll
    for (int i = 0; i < 4; ++i) sta4((xt ^ ((i & 1) * 64)) + (i >> 1) * 2048, d[i]);
    asm volatile("" ::: "memory");
  };
  if (T.late) {
    col_mats();
    if (f == 0) wait_vm<1>(); else if (f == 7) wait_vm<3>(); else wait_vm<0>();
    col256(ldsb + 3 * G0_STAGE, 1, false);
    const uint32_t xl = ldsb + 3 * G0_STAGE + f * 4096 + (lane >> 3) * 128 + ((lane ^ (lane >> 3)) & 7) * 16;
#pragma unroll
    for (int i = 0; i < 4; ++i) wv[i] = ldsa(xl + i * 1024);
    const int zo = (2 * f + 16 * (lane >> 3)) * 16384 + (li & 7) * 2048 + (li >> 3) * 128 + (lane & 7) * 16;
    asm volatile("" : "+v"(wv[0]), "+v"(wv[1]), "+v"(wv[2]), "+v"(wv[3]));
#pragma unroll
    for (int i = 0; i < 4; ++i) __builtin_amdgcn_raw_buffer_store_b128(wv[i], rzb, zo + (i >> 1) * 16384 + (i & 1) * (2 << 20), 0, 16);
    bars(2);
  } else {
    const char* tab = smem + L_X + lane * 16;
    m1.p = lds4(tab), m1.q = lds4(tab + 1024), m8.p = lds4(tab + 10240), m8.q = lds4(tab + 11264);
#pragma unroll
    for (int i = 0; i < 4; ++i) lds_cf2(tab + 2048 + i * 1024, tw[i >> 1][(i & 1) * 2], tw[i >> 1][(i & 1) * 2 + 1]);
  }
  wait_vm<0>();
  asm volatile("" : "+v"(epoch));
  const u64 tag = (epoch + 1) | (1ull << 63);
  u64 f1 = tag;
  asm volatile("" : "+v"(f1));
  bar();
  if (f == 0 && lane == 0) st_dev((u64*)(ws + WS_F1) + bid, f1);
  int nb = T.late ? 10 : 11;
  if (T.late && f >= 6) {
    uint64_t pbr = (uint64_t)A.Bre, pbi = (uint64_t)A.Bim;
    asm volatile("" : "+s"(pbr), "+s"(pbi));
    uint64_t pf = ((lane & 16) ? pbi : pbr) + (T.tn * 64 + (T.tm >> 2) * 16 + l15) * 2048 + ((2 * f + (lane >> 5) + k_rot()) & 15) * 128;
    asm volatile("" : "+v"(pf));
    bars(f - 4);
    nb -= f - 4;
    __builtin_amdgcn_s_sleep(5);
    uint32_t keep;
    asm volatile("s_mov_b32 %0, m0\n\ts_mov_b32 m0, %2\n\ts_nop 0\n\tglobal_load_lds_dword %1, off\n\ts_mov_b32 m0, %0"
                 : "=&s"(keep)
                 : "v"(pf), "s"(ldsb + L_SCR2)
                 : "memory");
  }
  cf plam, pcof, lp[11];
  if (f == 2) {
    const u16* raw = reinterpret_cast<const u16*>(smem + L_PRM + 512);
    uint32_t pr = raw[lane], pi = raw[64 + lane], pd = raw[128 + lane];
    asm volatile("" : "+v"(pr), "+v"(pi), "+v"(pd)::"memory");
    auto expf_ = [](float x) {
      const float t = x * 1.44269504088896341f;
      const float r = __builtin_fmaf(x, 1.44269504088896341f, -t) + x * 1.92596299112661746e-8f;
      const float e = __builtin_amdgcn_exp2f(t);
      return __builtin_fmaf(e * r, 0.693147180559945309f, e);
    };
    const float lr = u2f(pr << 16), li = u2f(pi << 16);
    float dt = rbf(expf_(u2f(pd << 16)));
    asm volatile("" : "+v"(dt));
    bar();
    const float zr = dt * lr, zi = dt * li;
    const float e = expf_(zr);
    const float rh = zi * 0.159154936671257019f;
    const float rf = (rh - __builtin_rintf(rh)) + __builtin_fmaf(zi, 6.42063832653e-9f, __builtin_fmaf(zi, 0.159154936671257019f, -rh));
    plam = cf{e * __builtin_amdgcn_cosf(rf), e * __builtin_amdgcn_sinf(rf)};
    asm volatile("" : "+v"(plam));
    bar();
    float den = 2.0f / (lr * lr + li * li);
    asm volatile("" : "+v"(den));
    bar();
    const float nr = plam.x - 1.0f, ni = plam.y;
    pcof = cf{(nr * lr + ni * li) * den, (ni * lr - nr * li) * den};
    lp[0] = cmul(plam, plam);
#pragma unroll
    for (int k = 1; k < 4; ++k) lp[k] = cmul(lp[k - 1], lp[k - 1]);
    asm volatile("" : "+v"(pcof), "+v"(lp[3]));
    bar();
#pragma unroll
    for (int k = 4; k < 10; ++k) lp[k] = cmul(lp[k - 1], lp[k - 1]);
    lp[10] = cmul(lp[8], lp[9]);
    asm volatile("" : "+v"(lp[4]), "+v"(lp[5]), "+v"(lp[6]), "+v"(lp[8]), "+v"(lp[9]), "+v"(lp[10]));
    nb -= 4;
  }
  bars(nb);
  u64 v[4];
  auto poll1 = [&]() {
#pragma unroll
    for (int i = 0; i < 4; ++i) v[i] = ld_dev((const u64*)(ws + WS_F1) + lane + 64 * i);
  };
  if (f == 0) {
    __builtin_amdgcn_s_sleep(5);
    poll1();
  }
  int zo = (q < 2 ? bid : (bid ? 256 - bid : 128)) * 16384 + (q & 1) * 8192 + f * 256 + l15 * 16;
  asm volatile("" : "+v"(zo));
  bar();
  if (T.late && f >= 4) {
    __builtin_amdgcn_s_sleep(6);
    warm1(smem, (f & 1) ? A.Cre : A.Cim, (T.nt * 32 + T.tm * 2 + ((f >> 1) & 1)) * 8192 + lane * 128);
  }
  if (f == 2) {
    if (T.late) lds_wait<SW_LD2, 1>(U, 4);
    auto pair = [&](int o, cf a, cf b) { sta4(ldsb + o + lane * 16, u32x4{f2u(a.x), f2u(a.y), f2u(b.x), f2u(b.y)}); };
    pair(L_PRM, plam, pcof);
    pair(L_PART + 512, lp[3], lp[4]), pair(L_PART + 1536, lp[5], lp[6]);
    pair(L_GRP, cf{1.f, 0.f}, lp[8]), pair(L_GRP + 1024, lp[9], lp[10]);
    wait_lgkm0();
    lds_signal<SW_LD2>(U);
  }
  if (f == 0) {
    __builtin_amdgcn_s_setprio(2);
#pragma nounroll
    for (unsigned spins = 0; spins < SPIN_MAX; ++spins) {
      bool ok = true;
#pragma unroll
      for (int i = 0; i < 4; ++i) ok &= v[i] == tag;
      if (__builtin_amdgcn_ballot_w64(!ok) == 0) break;
      __builtin_amdgcn_s_sleep(16);
      poll1();
    }
    lds_signal<SW_F1>(U);
    asm volatile("" ::"v"(v[0]), "v"(v[1]), "v"(v[2]), "v"(v[3]));
  }
  u32x4 zi[4];
  if (!T.late) {
    if (f) lds_wait<SW_F1, 1>(U, 1);
#pragma unroll
    for (int e = 0; e < 4; ++e) zi[e] = __builtin_amdgcn_raw_buffer_load_b128(rzb, zo + e * 2048, 0, 16);
  }
  if (f == 0) __builtin_amdgcn_s_setprio(0);
  if (f == 0 && lane == 0) {
    if (T.late) st_dev((uint32_t*)(ws + WS_P2) + bid, 256u);
    if (bid == 0) st_dev((u64*)(ws + WS_EPOCH), epoch + 1);
  }
  if ((bid | f) == 0) __builtin_amdgcn_s_setprio(3);
  if (!T.late) {
    const __amdgpu_buffer_rsrc_t rr = __builtin_amdgcn_make_buffer_rsrc((void*)A.w_r, 0, 2048 * 1026, 0x00020000);
    const __amdgpu_buffer_rsrc_t ri = __builtin_amdgcn_make_buffer_rsrc((void*)A.w_i, 0, 2048 * 1026, 0x00020000);
    uint32_t sr, si;
    auto wload = [&](int ka) {
      const int oa = ka * 1026, ob = ((2048 - ka) & 2047) * 1026;
      wv[0] = __builtin_amdgcn_raw_buffer_load_b128(rr, lane * 16, oa & ~3, 2);
      wv[1] = __builtin_amdgcn_raw_buffer_load_b128(ri, lane * 16, oa & ~3, 2);
      wv[2] = __builtin_amdgcn_raw_buffer_load_b128(rr, lane * 16, (ob + 2) & ~3, 2);
      wv[3] = __builtin_amdgcn_raw_buffer_load_b128(ri, lane * 16, (ob + 2) & ~3, 2);
      const int so = lane == 0 ? ob : (lane == 1 ? oa + 1024 : ((ka & 1) ? oa + 1022 : ob + 1024));
      sr = __builtin_amdgcn_raw_buffer_load_b16(rr, so, 0, 2);
      si = __builtin_amdgcn_raw_buffer_load_b16(ri, so, 0, 2);
    };
    const bool two = (bid | f) == 0;
    u32x4 w2r, w2i;
    uint32_t s2r, s2i;
    if (two) {
      wload(1024);
      w2r = wv[0], w2i = wv[1], s2r = sr, s2i = si;
    }
    wload(ks_of(f));
    __builtin_amdgcn_sched_barrier(0);
    {
      u32x4 o[4];
      asm volatile("" : "+v"(zi[0]), "+v"(zi[1]), "+v"(zi[2]), "+v"(zi[3]));
#pragma unroll
      for (int c = 0; c < 4; ++c) {
        const u32x4 a = u32x4{zi[0][c], zi[1][c], zi[2][c], zi[3][c]};
        const f32x4 re = mfma_b(a, m8.p, Z4), im = mfma_b(a, m8.q, Z4);
#pragma unroll
        for (int r = 0; r < 4; ++r) o[r][c] = pk_bf(re[r], im[r]);
      }
      const uint32_t oa = l15 * 2048 + f * 256 + ((4 * q) ^ (l15 & 7) ^ ((f & 2) << 1)) * 16;
#pragma unroll
      for (int r = 0; r < 4; ++r) sta4(ldsb + L_Z + (oa ^ (r * 16)), o[r]);
      wait_lgkm0();
      lds_signal<SW_FS>(U);
    }
    uint32_t wsp = 0;
    if (two) {
      sta4(ldsb + L_WS + lane * 16, w2r), sta4(ldsb + L_WS + 1024 + lane * 16, w2i);
      wsp = s2r | s2i << 16;
    }
    asm volatile("" : "+v"(wsp));
    lds_wait<SW_FS, 0>(U, 8);
    prio<1>(!two);
    const uint32_t row = ldsb + L_Z + f * 2048;
    u32x4 x[2][2];
    auto rows = [&]() {
      const uint32_t ra = row + q * 512 + ((l15 >> 2) ^ f ^ ((q & 1) << 2)) * 16 + (l15 & 3) * 4;
#pragma unroll
      for (int i = 0; i < 16; ++i) x[i >> 3][(i >> 2) & 1][i & 3] = *(lds_u32*)(size_t)((ra ^ ((i & 4) * 16)) + (i & 3) * 128 + (i >> 3) * 16384);
    };
    rows();
    if (two) x[1][0] = x[0][0] & 0xFFFF0000u, x[1][1] = x[0][1] & 0xFFFF0000u, x[0][0] &= 0xFFFFu, x[0][1] &= 0xFFFFu;
    asm volatile("" : "+v"(x[0][0]), "+v"(x[0][1]), "+v"(x[1][0]), "+v"(x[1][1])::"memory");
    row_head(x, m16, m1, tw, !two);
    wsp = (uint32_t)__builtin_amdgcn_update_dpp((int)(sr | si << 16), (int)wsp, 0x114, 0xF, 0x2, false);
    sta4(row + lane * 16, wv[0]);
    sta4(row + 1024 + lane * 16, wv[1]);
    if (!two) sta4(row + 16384 + lane * 16, wv[2]), sta4(row + 17408 + lane * 16, wv[3]);
    uint32_t wa = row + (bid & 1) * 2 + l15 * 2 + q * 256, wb = row + (two ? 0 : 16384) + 800 - (bid & 1) * 2 - l15 * 2 - q * 256;
    u32x4 w[4] = {u32x4{0u, 0u, 0u, 0u}, u32x4{0u, 0u, 0u, 0u}, u32x4{0u, 0u, 0u, 0u}, u32x4{0u, 0u, 0u, 0u}};
    asm volatile("" : "+v"(wsp), "+v"(w[0]), "+v"(w[1]), "+v"(w[2]), "+v"(w[3])::"memory");
    auto wread = [&](auto tc) {
      constexpr int t = decltype(tc)::value;
#pragma unroll
      for (int r = 0; r < 4; ++r) {
        uint32_t a = w[0][r], b = w[1][r], c = w[2][r], d = w[3][r];
        asm volatile("ds_read_u16_d16_hi %0, %4 offset:%6\n\tds_read_u16_d16_hi %1, %4 offset:%7\n\t"
                     "ds_read_u16_d16_hi %2, %5 offset:%8\n\tds_read_u16_d16_hi %3, %5 offset:%9"
                     : "+v"(a), "+v"(b), "+v"(c), "+v"(d)
                     : "v"(wa), "v"(wb), "n"(32 * t + 64 * r), "n"(32 * t + 64 * r + 1024), "n"(224 - 32 * t - 64 * r), "n"(1248 - 32 * t - 64 * r)
                     : "memory");
        w[0][r] = a, w[1][r] = b, w[2][r] = c, w[3][r] = d;
      }
    };
#pragma nounroll
    for (int jb = 0;; jb = 4) {
      wread(IC<0>{});
      const uint32_t s0 = __builtin_amdgcn_readlane(wsp, jb), s1 = __builtin_amdgcn_readlane(wsp, jb + 1);
      const uint32_t s2 = two ? s1 : __builtin_amdgcn_readlane(wsp, 63);
#pragma unroll
      for (int t = 0; t < 2; ++t) {
        cf u[4];
        lds_cf2(smem + L_X + 6144 + t * 2048 + lane * 16, u[0], u[1]), lds_cf2(smem + L_X + 7168 + t * 2048 + lane * 16, u[2], u[3]);
        f32x4 ar = mfma_b(m16.p, x[0][t], Z4), ai = mfma_b(m16.q, x[0][t], Z4), br = mfma_b(m16.p, x[1][t], Z4), bi = mfma_b(m16.q, x[1][t], Z4);
        auto half = [](uint32_t w, int hi) { return u2f(hi ? w & 0xFFFF0000u : w << 16); };
        __builtin_amdgcn_sched_barrier(0);
        asm volatile("s_waitcnt lgkmcnt(0)" : "+v"(w[0]), "+v"(w[1]), "+v"(w[2]), "+v"(w[3]), "+v"(u[0]), "+v"(u[1]), "+v"(u[2]), "+v"(u[3]));
        prio<0>(!two);
#pragma unroll
        for (int r = 0; r < 4; ++r) {
          cf fa = cf{u2f(w[0][r]), u2f(w[1][r])}, fb = cf{u2f(w[2][r]), -u2f(w[3][r])};
          if (t == 0 && r == 0 && lane == 0) {
            if (!(bid & 1)) fb = cf{half(s2, 0), -half(s2, 1)};
            fa = (fa + cf{half(s0, 0), -half(s0, 1)}) * 0.5f;
            fb = (fb + cf{half(s1, 0), half(s1, 1)}) * 0.5f;
          }
          if (t == 1 && r == 3 && (bid & 1) && lane == 63) fa = cf{half(s2, 0), half(s2, 1)};
          const cf X1 = cmad(u[r], cf{br[r], bi[r]}, cf{ar[r], ai[r]});
          const cf X2 = cf{__builtin_fmaf(2.f, ar[r], -X1.x), __builtin_fmaf(2.f, ai[r], -X1.y)};
          const cf Yb = cmul(X2, fb), S = fin(cmad(X1, fa, Yb));
          const cf V = fin(cmul(cf{u[r].x, -u[r].y}, cf{__builtin_fmaf(-2.f, Yb.x, S.x), __builtin_fmaf(-2.f, Yb.y, S.y)}));
          ar[r] = S.x, ai[r] = S.y, br[r] = V.x, bi[r] = V.y;
        }
        prio<1>(!two);
        if (t == 0) wread(IC<1>{});
        if (two) ar += br, ai += bi;
#pragma unroll
        for (int r = 0; r < 4; ++r) x[0][t][r] = pk_bf(ar[r], -ai[r]), x[1][t][r] = pk_bf(br[r], -bi[r]);
      }
      if (!two) break;
      if (jb) {
        x[1][0] = x[0][0], x[1][1] = x[0][1];
        x[0][0] = ldsa(row + lane * 16), x[0][1] = ldsa(row + 1024 + lane * 16);
        break;
      }
      sta4(row + lane * 16, x[0][0]), sta4(row + 1024 + lane * 16, x[0][1]);
      rows();
      x[0][0] = x[1][0] & 0xFFFFu, x[0][1] = x[1][1] & 0xFFFFu, x[1][0] &= 0xFFFF0000u, x[1][1] &= 0xFFFF0000u;
      row_head(x, m16, m1, tw, false);
      wa += L_WS - L_Z, wb += L_WS - L_Z;
    }
    row_head(x, m16, m1, tw, !two);
    const uint32_t oa = row + l15 * 128 + (q ^ (l15 & 7)) * 16;
#pragma unroll
    for (int i = 0; i < 4; ++i) {
      const f32x4 re = mfma_b(x[i >> 1][i & 1], m16.p, Z4), im = mfma_b(x[i >> 1][i & 1], m16.q, Z4);
      sta4((oa ^ ((i & 1) * 64)) + (i >> 1) * 16384, u32x4{pk_bf(re[0], im[0]), pk_bf(re[1], im[1]), pk_bf(re[2], im[2]), pk_bf(re[3], im[3])});
    }
    m8.p = lds4(smem + L_X + 12288 + lane * 16), m8.q = lds4(smem + L_X + 13312 + lane * 16);
    fsync(2);
    u32x4 yi[4];
#pragma unroll
    for (int e = 0; e < 4; ++e) yi[e] = lds4(smem + L_Z + (4 * q + e) * 2048 + f * 256 + (l15 ^ ((2 * f + (l15 >> 3)) & 7)) * 16);
    asm volatile("" : "+v"(yi[0]), "+v"(yi[1]), "+v"(yi[2]), "+v"(yi[3]));
    u32x4 o[4];
#pragma unroll
    for (int c = 0; c < 4; ++c) {
      const u32x4 y = u32x4{yi[0][c], yi[1][c], yi[2][c], yi[3][c]};
      const f32x4 re = mfma_b(m8.p, y, Z4), im = mfma_b(m8.q, y, Z4);
#pragma unroll
      for (int r = 0; r < 4; ++r) o[r][c] = pk_bf(re[r], im[r]);
    }
    const int zc = (int)(WS_ZC - WS_ZB) + q * 4096 + f * 256 + l15 * 16;
    asm volatile("" : "+v"(o[0]), "+v"(o[1]), "+v"(o[2]), "+v"(o[3]));
    __builtin_amdgcn_s_setprio(0);
#pragma unroll
    for (int i = 0; i < 4; ++i)
      __builtin_amdgcn_raw_buffer_store_b128(o[i], rzb, zc + ((i & 1) ? (bid ? 256 - bid : 128) : bid) * 16384 + (i >> 1) * 2048, 0, 16);
  } else {
    if (f >= 4) return true;
    if (f == 1) {
      lds_wait<SW_PUB, 4>(U, 4);
      lds_wait<SW_F1, 4>(U, 1);
      __builtin_amdgcn_s_sleep(10);
      poll_count<SW_G1>(ws, 4 * T.tm + 1 - (T.nt >> 2), 4, U);
    }
  }
  const int N = T.late ? 32 - NX : NX;
  if (T.late && f != 1) return false;
  bars(2);
  wait_vm<0>();
  bar();
  if (f == 1) {
    if (T.late) bars(2);
    {
      uint32_t c;
      asm volatile("s_mov_b64 exec, 1\n\tglobal_atomic_add %0, %1, %1, %2 sc0\n\ts_mov_b64 exec, -1\n\ts_barrier\n\ts_waitcnt vmcnt(0)"
                   : "=&v"(c)
                   : "v"(0), "s"(ws + WS_HF + (4 * T.tm + 2) * 128)
                   : "memory");
      if (__builtin_amdgcn_readfirstlane(c) < 8u) poll_count<SW_FR>(ws, 4 * T.tm + 2, 8, U);
    }
    if (T.late) {
      bars(N - 11);
      poll_count<SW_PX, true>(ws, 64 + li, 4, U);
      return false;
    }
    bars(NX - 8);
  } else if (f == 0) {
    if (lane == 0) st_dev((uint32_t*)(ws + WS_P2) + bid, 256u);
    bars(NX - 9);
    const __amdgpu_buffer_rsrc_t rp = __builtin_amdgcn_make_buffer_rsrc((void*)(ws + WS_P2), 0, 1024, 0x00020000);
    u32x4 pw = __builtin_amdgcn_raw_buffer_load_b128(rp, lane * 16, 0, 16);
    bars(2);
#pragma nounroll
    for (unsigned spins = 0; spins < SPIN_MAX; ++spins) {
      if (__builtin_amdgcn_ballot_w64((pw[0] & pw[1] & pw[2] & pw[3] & 256u) == 0) == 0) break;
      __builtin_amdgcn_s_sleep(8);
      pw = __builtin_amdgcn_raw_buffer_load_b128(rp, lane * 16, 0, 16);
      asm volatile("" ::: "memory");
    }
    lds_signal<SW_P2>(U);
    asm volatile("" ::"v"(pw));
    if (lane < 2) st_dev((u64*)(ws + WS_F1) + bid + lane * 128, 0ull);
  } else {
    bars(NX - 7);
  }
  {
    asm volatile("" : "+v"(lane));
    l15 = lane & 15;
    q = lane >> 4;
    constexpr uint32_t img = 256;
    const uint32_t vo = (2 * f + 16 * (lane >> 3)) * 16384 + (li & 7) * 2048 + (li >> 3) * 128 + ((lane & 7) ^ ((lane >> 5) * 4)) * 16;
    if (f) lds_wait<SW_P2, 1>(U, 1);
    gdev2(ws + WS_ZC, vo, vo + (2 << 20) - 1024, img + f * 4096);
    gdev2(ws + WS_ZC + 16384, vo, vo + (2 << 20) - 1024, img + f * 4096 + 2048);
    col_mats();
    wait_vm<0>();
    col256(img, 3, true);
    typedef __attribute__((address_space(3))) u32x2 lds_u32x2;
    const uint32_t xr = img + f * 4096 + q * 128 + ((l15 >> 1) ^ q) * 16 + (l15 & 1) * 8;
    u32x2 z[8];
#pragma unroll
    for (int i = 0; i < 8; ++i) z[i] = *(lds_u32x2*)(size_t)((xr ^ ((i & 1) * 64)) + i * 512);
    const __amdgpu_buffer_rsrc_t ro = __builtin_amdgcn_make_buffer_rsrc((void*)A.out, 0, 16 << 20, 0x00020000);
    const int oo = (2 * f + 16 * q) * 65536 + (li & 7) * 8192 + (li >> 3) * 256 + l15 * 16;
    lds_wait<SW_PL, 2>(U, 4);
#pragma unroll
    for (int i = 0; i < 8; ++i) {
      const uint32_t p0 = z[i][0], p1 = z[i][1];
      __builtin_amdgcn_raw_buffer_store_b128(u32x4{p0 << 16, p0 & 0xFFFF0000u, p1 << 16, p1 & 0xFFFF0000u}, ro, oo + (i >> 2) * 65536 + (i & 3) * (4 << 20), 0, 16);
    }
  }
  return false;
}

__device__ __forceinline__ void publish(char* ws, int idx, int lane) {
  wait_vm<0>();
  if (lane == 0) __hip_atomic_fetch_add((uint32_t*)(ws + WS_HF + idx * 128), 1u, __ATOMIC_RELAXED, __HIP_MEMORY_SCOPE_AGENT);
}
template <int N> __device__ __forceinline__ void publish8(char* ws, int idx) {
  uint32_t v, c;
  asm volatile(
      "s_waitcnt vmcnt(%5)\n\ts_mov_b64 exec, 1\n\tds_add_rtn_u32 %0, %2, %2 offset:%4\n\ts_waitcnt lgkmcnt(0)\n\tv_readfirstlane_b32 %1, %0\n\t"
      "s_cmp_lg_u32 %1, 28\n\ts_cbranch_scc1 1f\n\tv_mov_b32 %0, 1\n\tglobal_atomic_add %2, %0, %3 offset:-4\n1:\n\ts_mov_b64 exec, -1"
      : "=&v"(v), "=&s"(c)
      : "v"(4), "s"(ws + WS_HF + idx * 128), "n"(SW_HD * 4 - 4), "n"(N)
      : "memory", "scc");
}
__device__ __forceinline__ void states(char* ws, char* smem, int wave, int lane, int jp, cf (&v)[16], cf pw, cf lam) {
  const Tile T = tile_map();
  const int wr = wave >> 1, pl = (wave & 1) * 32 + jp * 16 + (lane & 15), fq = lane >> 4;
  const __amdgpu_buffer_rsrc_t rs = __builtin_amdgcn_make_buffer_rsrc((void*)(ws + WS_HS), 0, 8 << 20, 0x00020000);
  int hb = ((T.tm * 2 + wr) * 32 + T.tn * 2) * 8192 + fq * 2048 + pl * 16, hc = (hb ^ 64) + 1024;
  uint32_t hl = lds_base(smem) + G0_STAGE + wr * 8192 + (fq ^ g1_rot(wr)) * 2048 + pl * 16, hm = (hl ^ 64) + 1024;
  uint32_t ga = lds_base(smem) + L_GRP + pl * 16;
  int hb2 = hb + 8192, hc2 = hc + 8192;
  asm volatile("" : "+v"(hb), "+v"(hc), "+v"(hb2), "+v"(hc2), "+v"(hl), "+v"(hm), "+v"(ga));
  lds_wait<SW_GRP, 1>(4, 4);
  const u32x4 g0 = ldsa(ga), g1 = ldsa(ga + 1024);
  cf h = cmad(pw, (cf{u2f(g1[2]), u2f(g1[3])} + cf{u2f(g1[0]), u2f(g1[1])}) + (cf{u2f(g0[2]), u2f(g0[3])} + cf{u2f(g0[0]), u2f(g0[1])}), v[0]);
#pragma unroll
  for (int k = 0; k < 2; ++k) {
    u32x4 re, im;
#pragma unroll
    for (int i = 0; i < 4; ++i) {
      if (k | i) h = cmad(h, lam, v[8 * k + 2 * i]);
      const cf g = cmad(h, lam, v[8 * k + 2 * i + 1]);
      re[i] = pk_bf(h.x, g.x), im[i] = pk_bf(-h.y, -g.y);
      h = g;
    }
    __builtin_amdgcn_raw_buffer_store_b128(re, rs, k ? hc : hb, 0, 16);
    __builtin_amdgcn_raw_buffer_store_b128(im, rs, k ? hc2 : hb2, 0, 16);
    sta4(k ? hm : hl, re);
    sta4((k ? hm : hl) + G0_STAGE, im);
    __builtin_amdgcn_sched_barrier(0);
  }
  wait_lgkm0();
  lds_signal<SW_HS>(4);
}
__device__ __forceinline__ void loader_role(const Args& A, char* smem, int wave, int lane) {
  char* const ws = A.ws;
  const Tile T = tile_map();
  const int tm = T.tm, tn = T.tn;
  lds_u32* const sw = (lds_u32*)(smem + L_SYNC);
  const uint32_t U = (uint32_t)(size_t)(sw + 1);
  const uint32_t ldsb = lds_base(smem), lds0 = ldsb + wave * 4096;
  const int prow = lane >> 3;
  {
    __builtin_amdgcn_s_setprio(0);
    const uint32_t va = (tm * 128 + wave * 32 + prow) * 2048 + ((lane & 7) ^ (prow & 3)) * 16;
    const uint32_t vb = (tn * 64 + wave * 16 + prow) * 2048 + ((lane & 7) ^ prow) * 16;
    if (T.late) {
      bar();
      const int wc = (bid_x() >> 3) & 15;
      if ((wc & 7) != 3) {
#pragma nounroll
        for (int i = 2 + wave; i < 7; i += 4) warm_r(A, smem, lane, wc, i);
      }
      stage0(A, smem, wave, lane, 0);
    }
    stage0(A, smem, wave, lane, 1);
    {
      uint32_t k = 2, o = ((2 + k_rot()) & 15) * 128, so = 2 * G0_STAGE;
      const u64 bx = (u64)A.xB, br = (u64)A.Bre, bi = (u64)A.Bim - 2048;
      asm volatile(
          "s_add_u32 s88, %[xl], %[o]\n\ts_addc_u32 s89, %[xh], 0\n\ts_add_u32 m0, %[l0], %[so]\n1:\n\ts_waitcnt vmcnt(8)\n\ts_barrier\n\t"
          "global_load_lds_dwordx4 %[a0], s[88:89]\n\tglobal_load_lds_dwordx4 %[a1], s[88:89] offset:1024\n\t"
          "global_load_lds_dwordx4 %[a2], s[88:89] offset:2048\n\tglobal_load_lds_dwordx4 %[a3], s[88:89] offset:3072\n\t"
          "s_add_u32 m0, m0, 16384\n\ts_add_u32 s84, %[brl], %[o]\n\ts_addc_u32 s85, %[brh], 0\n\t"
          "s_add_u32 s86, %[bil], %[o]\n\ts_addc_u32 s87, %[bih], 0\n\ts_add_u32 %[o], %[o], 128\n\ts_and_b32 %[o], %[o], 0x780\n\t"
          "s_add_u32 s88, %[xl], %[o]\n\ts_addc_u32 s89, %[xh], 0\n\t"
          "s_add_u32 %[so], %[so], 0x8000\n\ts_and_b32 %[so], %[so], 0x18000\n\ts_add_u32 %[k], %[k], 1\n\t"
          "global_load_lds_dwordx4 %[b0], s[84:85]\n\tglobal_load_lds_dwordx4 %[b1], s[84:85] offset:1024\n\t"
          "global_load_lds_dwordx4 %[b0], s[86:87] offset:2048\n\tglobal_load_lds_dwordx4 %[b1], s[86:87] offset:3072\n\t"
          "s_add_u32 m0, %[l0], %[so]\n\ts_cmp_lt_u32 %[k], 16\n\ts_cbranch_scc1 1b"
          : [k] "+&s"(k), [o] "+&s"(o), [so] "+&s"(so)
          : [a0] "v"(va), [a1] "v"(va + 15360), [a2] "v"((va ^ 64) + 2 * 15360), [a3] "v"((va ^ 64) + 3 * 15360), [b0] "v"(vb), [b1] "v"(vb + 15360),
            [xl] "s"((uint32_t)bx), [xh] "s"((uint32_t)(bx >> 32)), [brl] "s"((uint32_t)br), [brh] "s"((uint32_t)(br >> 32)),
            [bil] "s"((uint32_t)bi), [bih] "s"((uint32_t)(bi >> 32)), [l0] "s"(lds0)
          : "memory", "scc", "s84", "s85", "s86", "s87", "s88", "s89"
      );
    }
    wait_vm<8>();
    lds_signal<SW_LD>(U);
    wait_vm<0>();
    lds_signal<SW_LD2>(U);
  }
  __builtin_amdgcn_s_setprio(2);
  {
    const int nseg = 2 * tm - 8 * wave;
    cf G = cf{0.f, 0.f};
    lds_wait<SW_LD2, 2>(U, 5);
    lds_wait<SW_F1, 2>(U, 1);
    cf lw = *reinterpret_cast<const cf*>(smem + L_GRP + (wave >> 1) * 1024 + lane * 16 + (wave & 1) * 8);
    if (nseg > 0) {
      const __amdgpu_buffer_rsrc_t ra = __builtin_amdgcn_make_buffer_rsrc((void*)(ws + WS_AGG), 0, 512 << 10, 0x00020000);
      int o[8];
#pragma unroll
      for (int k = 0; k < 8; ++k) {
        o[k] = ((nseg - 1 - k) * 1024 + tn * 64 + lane) * 16;
        asm volatile("" : "+v"(o[k]));
      }
      u32x4 u[8];
      cf l64, l128;
      lds_cf2(smem + L_PART + 1536 + lane * 16, l64, l128);
      asm volatile("" : "+v"(l64), "+v"(l128), "+v"(lw));
      lds_wait<SW_PUB, 1>(U, 4);
      __builtin_amdgcn_s_sleep(10);
#pragma nounroll
      for (unsigned spins = 0; spins < SPIN_MAX; ++spins) {
        uint32_t n = 0;
#pragma unroll
        for (int k = 0; k < 8; ++k) u[k] = __builtin_amdgcn_raw_buffer_load_b128(ra, o[k], 0, 16);
#pragma unroll
        for (int k = 0; k < 8; ++k) n += u[k][1] + u[k][3];
        if (__builtin_amdgcn_ballot_w64(n != 2u * (nseg < 8 ? nseg : 8)) == 0) break;
        __builtin_amdgcn_s_sleep(4);
        asm volatile("" ::: "memory");
      }
#pragma unroll
      for (int k = 6; k >= 0; k -= 2)
        G = cmad(G, l128, cmad(cf{u2f(u[k + 1][0]), u2f(u[k + 1][2])}, l64, cf{u2f(u[k][0]), u2f(u[k][2])}));
      G = cmul(G, lw);
    }
    *reinterpret_cast<cf*>(smem + L_GRP + (wave >> 1) * 1024 + lane * 16 + (wave & 1) * 8) = G;
    wait_lgkm0();
    lds_signal<SW_GRP>(U);
  }
  int tv = g1_kt(T.late, lane & 31) & 31;
  asm volatile("" : "+v"(tv));
  {
    lds_wait<SW_HO, 1>(U, 4);
    cf v[16];
#pragma unroll
    for (int i = 0; i < 8; ++i) lds_cf2(smem + (T.late ? 0 : 3 * G0_STAGE) + wave * 8192 + i * 1024 + lane * 16, v[2 * i], v[2 * i + 1]);
    cf pw = *reinterpret_cast<const cf*>(smem + L_TB + 2048 + wave * 512 + lane * 8);
    cf lam = reinterpret_cast<const cf*>(smem + L_PRM)[(wave & 1) * 64 + 32 + (lane & 15) * 2];
#pragma unroll
    for (int i = 0; i < 16; i += 4) asm volatile("" : "+v"(v[i]), "+v"(v[i + 1]), "+v"(v[i + 2]), "+v"(v[i + 3]));
    asm volatile("" : "+v"(pw), "+v"(lam));
    __builtin_amdgcn_s_setprio(3);
    states(ws, smem, wave, lane, 1, v, pw, lam);
    __builtin_amdgcn_s_setprio(2);
  }
  const char* ab = ws + WS_HS - 2048 + (size_t)tm * (64 * 8192);
  const uint32_t vh2 = ((wave >> 1) * 262144 + (wave & 1) * 4096 + 2048 + lane * 16) ^ (g1_rot(wave >> 1) * 2048), vh = (vh2 ^ 2048) + 2048;
  auto h2 = [&]() {
    const char* pa = ab + g1_kt(T.late, 2) * 8192;
    glds2_dev(pa, vh, lds0 + 3 * G0_STAGE);
    glds2_dev(pa + 2048, vh2, lds0 + 3 * G0_STAGE + 2048);
  };
  bool got = false;
  if (!T.late) {
    stage_c(A, wave, lane, 0);
    stage_c(A, wave, lane, 1);
    publish8<8>(ws, 4 * tm + (T.nt >> 2));
    wait_vm<0>();
    lds_signal<SW_CP>(U);
    if (wave == 0) poll_count<SW_G1, false, true>(ws, 4 * tm + (T.nt >> 2), 4, U, ab + g1_kt(0, 2) * 8192, vh, vh2, lds0 + 3 * G0_STAGE);
    else lds_wait<SW_HS, 1>(U, 8);
    got = wave == 0;
    stage_c(A, wave, lane, 2);
  } else {
    got = lds_peek<SW_G1>(U) != 0;
    if (got) {
      h2();
      publish8<4>(ws, 4 * tm + 2);
    } else {
      publish8<0>(ws, 4 * tm + 2);
    }
  }
  if (!got) {
    lds_wait<SW_G1, 1>(U, 1);
    h2();
  }
  __builtin_amdgcn_s_setprio(0);
  const int N = T.late ? 32 - NX : NX;
  const uint32_t vc = g1_vc(wave, lane);
  lds_wait<SW_S0, 1>(U, 4);
  {
    uint32_t k = 3, kt = g1_kt(T.late, 3), dst = lds0 + G0_STAGE, t;
    asm volatile(
        "s_lshl_b32 %[t], %[kt], 13\n\ts_add_u32 s86, %[al], %[t]\n\ts_addc_u32 s87, %[ah], 0\n\t"
        "s_mov_b32 m0, %[dst]\n\ts_branch 2f\n1:\n\ts_waitcnt vmcnt(8)\n\ts_barrier\n2:\n\t"
        "global_load_lds_dwordx4 %[h0], s[86:87] sc1\n\tglobal_load_lds_dwordx4 %[h0], s[86:87] offset:1024 sc1\n\t"
        "global_load_lds_dwordx4 %[h1], s[86:87] offset:2048 sc1\n\tglobal_load_lds_dwordx4 %[h1], s[86:87] offset:3072 sc1\n\t"
        "s_add_u32 m0, m0, 16384\n\ts_bitcmp0_b32 %[kt], 0\n\ts_cselect_b64 s[84:85], %[cr], %[ci]\n\ts_lshl_b32 %[t], %[kt], 6\n\t"
        "s_and_b32 %[t], %[t], 0x780\n\ts_add_u32 s84, s84, %[t]\n\ts_addc_u32 s85, s85, 0\n\ts_add_u32 %[k], %[k], 1\n\t"
        "v_readlane_b32 %[kt], %[tv], %[k]\n\ts_add_u32 %[t], %[dst], 0x8000\n\ts_cmp_eq_u32 %[dst], %[dl]\n\ts_cselect_b32 %[dst], %[df], %[t]\n\t"
        "s_lshl_b32 %[t], %[kt], 13\n\ts_add_u32 s86, %[al], %[t]\n\ts_addc_u32 s87, %[ah], 0\n\t"
        "global_load_lds_dwordx4 %[c0], s[84:85]\n\tglobal_load_lds_dwordx4 %[c1], s[84:85] offset:1024\n\t"
        "global_load_lds_dwordx4 %[c0], s[84:85] offset:2048\n\tglobal_load_lds_dwordx4 %[c1], s[84:85] offset:3072\n\t"
        "s_mov_b32 m0, %[dst]\n\ts_cmp_lt_u32 %[k], %[n]\n\ts_cbranch_scc1 1b"
        : [k] "+&s"(k), [kt] "+&s"(kt), [dst] "+&s"(dst), [t] "=&s"(t)
        : [h0] "v"(vh), [h1] "v"(vh2), [c0] "v"(vc), [c1] "v"(vc + 65536 - 1024), [tv] "v"(tv), [cr] "s"(A.Cre), [ci] "s"(A.Cim),
          [al] "s"((uint32_t)(u64)ab), [ah] "s"((uint32_t)((u64)ab >> 32)), [df] "s"(lds0 + G0_STAGE), [dl] "s"(lds0 + 3 * G0_STAGE), [n] "s"(N)
        : "memory", "scc", "s84", "s85", "s86", "s87"
    );
  }
  wait_vm<8>();
  lds_signal<SW_LD>(U);
  wait_vm<0>();
  lds_signal<SW_LD2>(U);
  if (T.late) {
    const char* ps = ws + WS_PS + (size_t)(bid_x() & 127) * 32768 + wave * 8192;
    lds_wait<SW_PX, 1>(U, 1);
#pragma nounroll
    for (int i = 0; i < 4; ++i) glds2_dev(ps + i * 2048, lane * 16, ldsb + wave * 8192 + i * 2048);
    wait_vm<0>();
    lds_signal<SW_PL>(U);
  }
}

template <bool TR> struct Core {
  struct AF {
    u32x2 l, h;
  };
  f32x4 (&acc)[4][4];
  uint32_t pa0, pa1, pb0, pb1;
  AF a[2][2];
  int so = 0;
  u32x4 b[4][2];
  static constexpr int DA = TR ? 2048 : 512;
  static constexpr int pn(int i) { return (i >> 3) == 1 ? 3 - ((i >> 1) & 3) : (i >> 1) & 3; }
  static constexpr int pk(int i) { return (i ^ (i >> 1)) & 1; }
  __device__ __forceinline__ void ra(int m, int j) {
    AF& d = a[m & 1][j >> 1];
    if (TR) {
      if (j & 1) d.h = lds_tr(pa1 + m * DA + (j >> 1) * 512); else d.l = lds_tr(pa0 + m * DA + (j >> 1) * 512);
    } else if ((j & 1) == 0) {
      const u32x4 v = ldsa((j ? pa1 : pa0) + m * DA);
      d.l = u32x2{v[0], v[1]}, d.h = u32x2{v[2], v[3]};
    }
    __builtin_amdgcn_sched_barrier(0);
  }
  __device__ __forceinline__ void rb(int i) {
    b[pn(i)][pk(i)] = ldsa((pk(i) ? pb1 : pb0) + pn(i) * 2048);
    __builtin_amdgcn_sched_barrier(0);
  }
  __device__ __forceinline__ void mm(int i) {
    const AF& x = a[(i >> 3) & 1][pk(i)];
    const u32x4 av = u32x4{x.l[0], x.l[1], x.h[0], x.h[1]};
    asm volatile("v_mfma_f32_16x16x32_bf16 %0, %1, %2, %0" : "+v"(acc[i >> 3][pn(i)]) : "v"(av), "v"(b[pn(i)][pk(i)]));
    __builtin_amdgcn_sched_barrier(0);
    if (i < 4) ra(1, i);
    if (i >= 8 && i < 12) ra(2, i - 8);
    if (i >= 15 && i < 19) ra(3, i - 15);
  }
  static __device__ __forceinline__ void add(uint32_t& p, int adv) {
    p += adv;
    asm volatile("" : "+v"(p));
    __builtin_amdgcn_sched_barrier(0);
  }
  template <int BAR> __device__ __forceinline__ void step(int& k, int k1, uint32_t cnt) {
    int adv;
    uint32_t cv, ca, ct;
#pragma unroll
    for (int i = 0; i < 32; ++i) {
      mm(i);
      if (i == 12)
        asm volatile("s_cmp_eq_u32 %1, %2\n\ts_cselect_b32 %0, %3, %4" : "=s"(adv) : "s"(so), "n"((TR ? 2 : 3) * G0_STAGE), "s"((TR ? -2 : -3) * G0_STAGE), "s"(G0_STAGE) : "scc");
      if (i == 13) asm volatile("s_add_u32 %0, %0, %2\n\ts_add_u32 %1, %1, 1" : "+s"(so), "+s"(k) : "s"(adv) : "scc");
      if (i == 14) add(pb0, adv);
      if (i == 19) add(pa0, adv);
      if (i == 20) add(pa1, adv);
      if (i == 21) add(pb1, adv);
      if (i == 17 && !BAR)
        asm volatile("s_sub_u32 %1, %2, %3\n\ts_lshl_b32 %1, %1, 2\n\tv_mov_b32 %0, %1\n\ts_add_u32 %1, %1, %4" : "=v"(ca), "=&s"(ct) : "s"(k), "s"(k1), "s"(cnt) : "scc");
      if (i == 20 && !BAR) asm volatile("ds_read_b32 %0, %1 offset:%2" : "=v"(cv) : "v"(ca), "n"(SW_LD * 4) : "memory");
      if (i == 23) __builtin_amdgcn_s_waitcnt(0xC07F);
      if (i == 23 && BAR == 1) asm volatile("s_barrier" ::: "memory");
      if (i == 23 && BAR == 2) {
        wait_vm<0>();
        lds_signal<SW_S0>(4);
        __builtin_amdgcn_s_setprio(1);
        __builtin_amdgcn_sched_barrier(0);
      }
      if (i == 23 && !BAR) {
        uint32_t c;
        asm volatile(
            "1:\n\ts_waitcnt lgkmcnt(0)\n\tv_readfirstlane_b32 %1, %0\n\tds_read_b32 %0, %2 offset:%4\n\t"
            "s_cmp_lt_u32 %1, %3\n\ts_cbranch_scc1 1b"
            : "+v"(cv), "=&s"(c)
            : "v"(ca), "s"(ct), "n"(SW_LD * 4)
            : "scc", "memory");
      }
      __builtin_amdgcn_sched_barrier(0);
      if (i == 24) rb(i);
      if (i >= 24 && i < (TR ? 28 : 26)) ra(0, TR ? i - 24 : 2 * (i - 24));
      if (i > 24) rb(i);
      if (i == 31 && !BAR) asm volatile("s_waitcnt lgkmcnt(%1)" ::"v"(cv), "n"(TR ? 12 : 10));
    }
  }
  __device__ __forceinline__ void run(int k, int n, uint32_t cnt) {
    const int k1 = k + n - 3;
    asm volatile("" : "+v"(pa0), "+v"(pa1), "+v"(pb0), "+v"(pb1), "+s"(so));
    if (!TR) bar_lds();
    ra(0, 0), ra(0, 1), rb(0), ra(0, 2), ra(0, 3);
#pragma unroll
    for (int i = 1; i < 8; ++i) rb(i);
    if (TR) {
#pragma nounroll
      do step<2>(k, 0, 0); while (k == 0);
    }
#pragma nounroll
    do step<1>(k, 0, 0); while (k != k1);
#pragma nounroll
    do step<0>(k, k1 + 1, cnt); while (k != k1 + 2);
  }
};

__device__ __forceinline__ void mfma_role(const Args& A, char* smem, int wave, int lane) {
  char* const ws = A.ws;
  const Tile T = tile_map();
  const int tm = T.tm, tn = T.tn;
  lds_u32* const sw = (lds_u32*)(smem + L_SYNC);
  const uint32_t U = (uint32_t)(size_t)(sw + 1);
  const int wr = wave >> 1, wc = wave & 1;
  int fr = lane & 15, fq = lane >> 4;
  {
    if (T.late) bar_lds();
    if (wave == 3) wait_vm<0>();
    __builtin_amdgcn_s_setprio(1);
  }
  {
    f32x4 acc[4][4];
#pragma unroll
    for (int m = 0; m < 4; ++m)
#pragma unroll
      for (int n = 0; n < 4; ++n) acc[m][n] = Z4;
    const cf* prm = reinterpret_cast<const cf*>(smem + L_PRM) + wc * 64;
    const cf* pw2 = reinterpret_cast<const cf*>(smem + L_PART + 512) + wc * 64;
    cf* part = reinterpret_cast<cf*>(smem + L_PART) + wc * 32;
    cf lam[2], l16[2], l32[2], s[2];
    int ao;
    const __amdgpu_buffer_rsrc_t rag = __builtin_amdgcn_make_buffer_rsrc((void*)(ws + WS_AGG), 0, 512 << 10, 0x00020000);
    auto scan = [&](int i, int jp) {
      const int m = i >> 2;
      if ((i & 3) == 0 || i == 1) asm volatile("" : "+v"(acc[m][2 * jp]), "+v"(acc[m][2 * jp + 1]));
      if (i == 1) s[jp] = cf{acc[0][2 * jp][0], acc[0][2 * jp + 1][0]};
      s[jp] = cmad(s[jp], lam[jp], cf{acc[m][2 * jp][i & 3], acc[m][2 * jp + 1][i & 3]});
      __builtin_amdgcn_sched_barrier(0);
    };
    {
      Core<false> g{acc};
      g.pa0 = lds_base(smem) + (wr * 64 + (fr >> 2) * 16 + (fr & 3)) * 128 + (fq ^ (fr & 7)) * 16;
      g.pb0 = lds_base(smem) + (128 + wc * 64 + fr) * 128 + (fq ^ (fr & 7)) * 16;
      g.pa1 = g.pa0 ^ 64, g.pb1 = g.pb0 ^ 64;
#pragma unroll
      for (int m = 0; m < 4; ++m) asm volatile("" : "+v"(acc[m][0]), "+v"(acc[m][1]), "+v"(acc[m][2]), "+v"(acc[m][3]));
      g.run(0, 16, 16);
#pragma unroll
      for (int i = 0; i < 32; ++i) {
        g.mm(i);
        if (i == 1) {
          asm volatile("" : "+v"(lane));
          lam[0] = prm[(lane & 15) * 2], lam[1] = prm[32 + (lane & 15) * 2];
          __builtin_amdgcn_s_setprio(3);
          __builtin_amdgcn_sched_barrier(0);
        }
        if (i >= 8 && i < 14) scan(1 + (i - 8) % 3, (i - 8) / 3);
        if (i >= 14 && i < 22) scan(4 + ((i - 14) & 3), i < 18);
        if (i >= 22) scan(8 + ((i - 22) & 3) + (i >= 30 ? 4 : 0), i >= 26 && i < 30);
        if (i == 25) {
          lds_signal<SW_G0E>(U);
          asm volatile("" : "+v"(lane));
          fr = lane & 15;
          fq = lane >> 4;
#pragma unroll
          for (int jp = 0; jp < 2; ++jp) lds_cf2(pw2 + jp * 32 + fr * 2, l16[jp], l32[jp]);
          ao = ((2 * tm + wr) * 1024 + tn * 64 + wc * 32 + lane) * 16 + ((lane & 32) << 15);
          asm volatile("" : "+v"(ao));
          __builtin_amdgcn_sched_barrier(0);
        }
      }
      scan(14, 0), scan(15, 0);
#pragma unroll
      for (int i = 12; i < 16; ++i) scan(i, 1);
    }
    const uint64_t m1 = 0xFFFF0000FFFF0000ull, m2 = 0xFFFFFFFF00000000ull;
    cf pe[2], a01[2], wg[2];
#pragma unroll
    for (int jp = 0; jp < 2; ++jp) {
      const u32x2 ex = __builtin_amdgcn_permlane16_swap(f2u(s[jp].x), f2u(s[jp].x), false, false);
      const u32x2 ey = __builtin_amdgcn_permlane16_swap(f2u(s[jp].y), f2u(s[jp].y), false, false);
      pe[jp] = cf{u2f(ex[0]), u2f(ey[0])};
      const cf ap = cmad(pe[jp], l16[jp], cf{u2f(ex[1]), u2f(ey[1])});
      const u32x2 hx = __builtin_amdgcn_permlane32_swap(f2u(ap.x), f2u(ap.x), false, false);
      const u32x2 hy = __builtin_amdgcn_permlane32_swap(f2u(ap.y), f2u(ap.y), false, false);
      a01[jp] = cf{u2f(hx[0]), u2f(hy[0])};
      wg[jp] = cmad(a01[jp], l32[jp], cf{u2f(hx[1]), u2f(hy[1])});
    }
    {
      const cf ws_ = sel_c(m1, wg[1], wg[0]);
      __builtin_amdgcn_raw_buffer_store_b128(u32x4{f2u(ws_.x), 1u, f2u(ws_.y), 1u}, rag, ao, 0, 16);
      if (wr == 0) {
        part[lane & 31] = ws_;
        wait_lgkm0();
      }
      lds_signal<SW_PUB>(U);
      if (wr) lds_wait<SW_PUB, 0>(U, 4);
    }
    __builtin_amdgcn_s_setprio(2);
    cf v[16], pw;
#pragma unroll
    for (int jp = 1; jp >= 0; --jp) {
      const cf cof = prm[jp * 32 + fr * 2 + 1];
      const cf p1 = sel_c(m1, l16[jp], cf{1.f, 0.f});
      cf cc = sel_c(m1, pe[jp], cf{0.f, 0.f});
      cc = sel_c(m2, cmad(p1, a01[jp], cc), cc);
      cf p = sel_c(m2, cmul(p1, l32[jp]), p1);
      if (wr) {
        cc = cmad(p, part[jp * 16 + fr], cc);
        p = cmul(p, pw2[128 + jp * 32 + fr * 2]);
      }
      pw = cmul(cmul(p, lam[jp]), cof);
#pragma unroll
      for (int i = 0; i < 16; ++i) {
        const cf u = cf{acc[i >> 2][2 * jp][i & 3], acc[i >> 2][2 * jp + 1][i & 3]};
        v[i] = cmul(i ? u : cmad(cc, lam[jp], u), cof);
        asm volatile("" : "+v"(v[i]));
      }
      asm volatile("" : "+v"(pw));
      if (jp) {
        lds_wait<SW_G0E, 0>(U, 4);
#pragma unroll
        for (int i = 0; i < 8; ++i)
          sta4(lds_base(smem) + (T.late ? 0 : 3 * G0_STAGE) + wave * 8192 + i * 1024 + lane * 16,
               u32x4{f2u(v[2 * i].x), f2u(v[2 * i].y), f2u(v[2 * i + 1].x), f2u(v[2 * i + 1].y)});
        *reinterpret_cast<cf*>(smem + L_TB + 2048 + wave * 512 + lane * 8) = pw;
        wait_lgkm0();
        lds_signal<SW_HO>(U);
      }
    }
    __builtin_amdgcn_s_setprio(3);
    states(ws, smem, wave, lane, 0, v, pw, lam[0]);
  }
  if (T.late) __builtin_amdgcn_s_setprio(0);
  publish8<0>(ws, 4 * tm + (T.late ? 2 : T.nt >> 2));
  if (T.late) {
    bar();
    bar();
  }
}

__device__ __forceinline__ void gemm1(const Args& A, char* smem, int wave, int lane) {
  char* const ws = A.ws;
  const Tile T = tile_map();
  const int tm = T.tm, nt = T.nt;
  const uint32_t U = 4;
  const int wr = wave >> 1, wc = wave & 1, fr = lane & 15, fq = lane >> 4;
  f32x4 acc[4][4];
  if (T.late) {
    const char* px = reinterpret_cast<const char*>(A.xB + tm * 128 * 1024 + nt * 128 + wc * 64);
    const uint32_t pxo = (wr * 64 + g1_rot(wr) * 16 + fq * 4) * 2048 + fr * 8;
    const u32x2 dw = reinterpret_cast<const u32x2*>(A.Dsk + nt * 128 + wc * 64)[fr];
    u32x2 xw[16];
#pragma unroll
    for (int i = 0; i < 16; ++i) xw[i] = *reinterpret_cast<const u32x2*>(px + ((pxo ^ ((i >> 2) << 15)) + (i & 3) * 2048));
#pragma unroll
    for (int i = 0; i < 32; ++i) {
      const uint32_t a = xw[i >> 1][i & 1], d = dw[i & 1];
      const uint32_t p = pk_bf(u2f(a << 16) * u2f(d << 16), u2f(a & 0xFFFF0000u) * u2f(d & 0xFFFF0000u));
      acc[i >> 3][(i & 1) * 2][(i >> 1) & 3] = u2f(p << 16);
      acc[i >> 3][(i & 1) * 2 + 1][(i >> 1) & 3] = u2f(p & 0xFFFF0000u);
      if (i == 15) {
#pragma unroll
        for (int m = 0; m < 2; ++m) asm volatile("" : "+v"(acc[m][0]), "+v"(acc[m][1]), "+v"(acc[m][2]), "+v"(acc[m][3]));
#pragma unroll
        for (int j = 8; j < 16; j += 4) asm volatile("" : "+v"(xw[j]), "+v"(xw[j + 1]), "+v"(xw[j + 2]), "+v"(xw[j + 3]));
        __builtin_amdgcn_sched_barrier(0);
        lds_wait<SW_G0E, 4>(U, 4);
        stage_c(A, wave, lane, 0);
        stage_c(A, wave, lane, 1);
        __builtin_amdgcn_sched_barrier(0);
      }
    }
#pragma unroll
    for (int m = 0; m < 4; ++m) asm volatile("" : "+v"(acc[m][0]), "+v"(acc[m][1]), "+v"(acc[m][2]), "+v"(acc[m][3]));
    wait_vm<0>();
    lds_signal<SW_CP>(U);
    stage_c(A, wave, lane, 2);
  } else {
#pragma unroll
    for (int i = 0; i < 16; ++i) acc[i >> 2][i & 3] = Z4;
  }
  {
    Core<true> g{acc};
    g.pa0 = lds_base(smem) + G0_STAGE + wr * 8192 + ((fr >> 1) & 1) * 1024 + (8 * fq + (fr >> 2) + (fr & 2) * 2) * 16 + (fr & 1) * 8;
    g.pa1 = g.pa0 ^ 64;
    g.pb0 = lds_base(smem) + G0_STAGE + 16384 + (wc * 64 + fr) * 128 + (fq ^ (fr & 7)) * 16;
    g.pb1 = g.pb0 ^ 64;
#pragma unroll
    for (int m = 0; m < 4; ++m) asm volatile("" : "+v"(acc[m][0]), "+v"(acc[m][1]), "+v"(acc[m][2]), "+v"(acc[m][3]));
    lds_wait<SW_CP, 0>(U, 4);
    lds_wait<SW_HS, 0>(U, 8);
    __builtin_amdgcn_s_setprio(0);
    g.run(0, T.late ? 32 - NX : NX, 32);
    const __amdgpu_buffer_rsrc_t rp = __builtin_amdgcn_make_buffer_rsrc((void*)(ws + WS_PS), 0, 8 << 20, 0x00020000);
    int po = bid_x() * 32768 + wave * 8192 + lane * 16;
    asm volatile("" : "+v"(po));
    auto part = [&](int m) {
      if (T.late) return;
      asm volatile("" : "+v"(acc[m][0]), "+v"(acc[m][1]), "+v"(acc[m][2]), "+v"(acc[m][3]));
#pragma unroll
      for (int i = 0; i < 2; ++i) {
        const f32x4 a = acc[m][2 * i], b = acc[m][2 * i + 1];
        __builtin_amdgcn_raw_buffer_store_b128(u32x4{pk_bf(a[0], b[0]), pk_bf(a[1], b[1]), pk_bf(a[2], b[2]), pk_bf(a[3], b[3])}, rp,
                                               po + (2 * m + i) * 1024, 0, 16);
      }
      __builtin_amdgcn_sched_barrier(0);
    };
#pragma unroll
    for (int i = 0; i < 32; ++i) {
      g.mm(i);
      if (i >= 12 && (i & 7) == 4) part((i >> 3) - 1);
    }
    asm volatile("s_nop 7" : "+v"(acc[3][0]), "+v"(acc[3][1]), "+v"(acc[3][2]), "+v"(acc[3][3]));
    part(3);
  }
  asm volatile("" : "+v"(lane));
  if (!T.late) {
    lds_signal<SW_PL>(U);
    publish(ws, 64 + bid_x(), lane);
    warm_dev(smem, ws + WS_PS, bid_x() * 32768 + wave * 8192 + lane * 128);
  } else {
    const __amdgpu_buffer_rsrc_t ro = __builtin_amdgcn_make_buffer_rsrc((void*)A.out, 0, 16 << 20, 0x00020000);
    const int o = ((tm * 128 + wr * 64 + g1_rot(wr) * 16 + (lane >> 4) * 4) * 2048 + 1024 + nt * 128 + wc * 64 + (lane & 15) * 4) * 4;
    const uint32_t pp = lds_base(smem) + wave * 8192 + lane * 16;
    lds_wait<SW_PL, 0>(U, 4);
    u32x4 p[8];
#pragma unroll
    for (int i = 0; i < 8; ++i) p[i] = ldsa(pp + i * 1024);
#pragma unroll
    for (int m = 0; m < 4; ++m) {
      u32x4 y[4];
#pragma unroll
      for (int i = 0; i < 16; ++i) {
        const int n = i & 3, r = i >> 2;
        const uint32_t w = p[2 * m + (n >> 1)][r];
        y[r][n] = f2u(acc[m][n][r] + u2f((n & 1) ? w & 0xFFFF0000u : w << 16));
      }
      asm volatile("" : "+v"(y[0]), "+v"(y[1]), "+v"(y[2]), "+v"(y[3]));
#pragma unroll
      for (int r = 0; r < 4; ++r) __builtin_amdgcn_raw_buffer_store_b128(y[r], ro, (o ^ (m << 17)) + r * 8192, 0, 16);
      __builtin_amdgcn_sched_barrier(0);
    }
  }
}

}

extern "C" __global__ __launch_bounds__(1024, 4) void k_all(const uint32_t* x2, const unsigned short* w_r, const unsigned short* w_i,
                                                           const unsigned short* xB, const unsigned short* Lre,
                                                           const unsigned short* Lim, const unsigned short* Bre,
                                                           const unsigned short* Bim, const unsigned short* Cre,
                                                           const unsigned short* Cim, const unsigned short* ldt,
                                                           const unsigned short* Dsk, float* out, char* ws) {
  __shared__ __attribute__((aligned(256))) char smem_[LDS_BYTES];
  char* const smem = smem_ + 256;
  const Args A{x2, w_r, w_i, xB, Lre, Lim, Bre, Bim, Cre, Cim, ldt, Dsk, out, ws};
  const int tid = tid_x(), lane = tid & 63;
  int wave = __builtin_amdgcn_readfirstlane(tid >> 6);
  const uint32_t ldsb = lds_base(smem);
  {
    const int o = lane * 128 + (lane >> 5) * ((bid_x() << 9) & 0x7000);
    asm volatile(
        "s_cmp_lg_u32 %1, %3\n\ts_cbranch_scc1 1f\n\ts_getpc_b64 s[90:91]\n\ts_add_u32 s90, s90, k_all@rel32@lo+4\n\t"
        "s_addc_u32 s91, s91, k_all@rel32@hi+12\n\ts_mov_b32 m0, %2\n\ts_nop 0\n\tglobal_load_lds_dword %0, s[90:91]\n1:"
        ::"v"(o < 34688 - 16 ? o : 34688 - 16), "s"(wave), "s"(ldsb + L_SCR2), "n"(0)
        : "s90", "s91", "scc", "memory");
  }
  if (wave < 8) {
    const uint32_t o = (2 * wave + 16 * (lane >> 3)) * 16384 + (bid_x() & 7) * 2048 + ((bid_x() >> 3) & 15) * 128 + ((lane & 7) ^ ((lane >> 5) * 4)) * 16;
    if (bid_x() >= 128)
      asm volatile(
          "s_mov_b32 m0, %4\n\ts_nop 0\n\tglobal_load_lds_dwordx4 %0, %2 nt\n\tglobal_load_lds_dwordx4 %1, %2 offset:1024 nt\n\t"
          "global_load_lds_dwordx4 %0, %3 offset:2048 nt\n\tglobal_load_lds_dwordx4 %1, %3 offset:3072 nt"
          ::"v"(o), "v"(o + (2 << 20) - 1024), "s"(x2), "s"(x2 + (16384 - 2048) / 4), "s"(ldsb + 3 * G0_STAGE + wave * 4096)
          : "memory");
    else
      stage0(A, smem, wave & 3, lane, 0, 1 + (wave >> 2));
  } else if (wave < 12) {
    const int wc = (bid_x() >> 3) & 15;
    if (bid_x() >= 128 && (wc & 7) != 3 && wave != 10)
      warm_r(A, smem, lane, wave == 11 ? (wc & 8) | 3 : wc, wave == 11 ? (wc & 7) - ((wc & 7) > 3) : wave - 8);
  } else if (wave == 15) {
    const uint32_t o = tile_map().tn * 128 + (lane & 31) * 4;
    asm volatile(
        "s_mov_b32 m0, %4\n\ts_mov_b32 exec_hi, 0\n\tglobal_load_lds_dword %0, %1\n\ts_add_u32 m0, m0, 128\n\ts_nop 0\n\t"
        "global_load_lds_dword %0, %2\n\ts_add_u32 m0, m0, 128\n\ts_nop 0\n\tglobal_load_lds_dword %0, %3\n\ts_mov_b32 exec_hi, -1"
        ::"v"(o), "s"(Lre), "s"(Lim), "s"(ldt), "s"(ldsb + L_PRM + 512)
        : "memory", "scc");
  } else if (wave == 13) {
    warm1(smem, Dsk, (lane & 15) * 128);
  }
  asm volatile("" : "+s"(wave));
  bool g1 = false;
  if (__builtin_expect(wave >= 12, 0)) {
    if (wave == 12) *(volatile lds_u32*)((lds_u32*)(smem + L_SYNC) + lane) = 0;
    mfma_role(A, smem, wave - 12, lane);
    g1 = bid_x() < 128;
  } else if (__builtin_expect(wave >= 8, 0)) {
    loader_role(A, smem, wave - 8, lane);
    __builtin_amdgcn_s_waitcnt(0x0F70);
  } else {
    g1 = fft_role(A, smem, wave, lane);
    __builtin_amdgcn_s_waitcnt(0x0F70);
  }
  if (g1) gemm1(A, smem, wave & 3, (int)__builtin_amdgcn_mbcnt_hi(~0u, __builtin_amdgcn_mbcnt_lo(~0u, 0u)));
}
#if defined(__HIP_DEVICE_COMPILE__) && __HIP_DEVICE_COMPILE__
#pragma clang attribute pop
#endif

extern "C" void kernel_launch(void* const* d_in, const int* in_sizes, int n_in, void* d_out, int out_size, void* d_ws,
                              size_t ws_size, hipStream_t stream) {
  typedef const unsigned short* P;
  k_all<<<256, 1024, 0, stream>>>((const uint32_t*)d_in[0], (P)d_in[1], (P)d_in[2], (P)d_in[3], (P)d_in[4], (P)d_in[5], (P)d_in[6],
                                  (P)d_in[7], (P)d_in[8], (P)d_in[9], (P)d_in[10], (P)d_in[11], (float*)d_out, (char*)d_ws);
}
```
